```python
import torch

def _softmax(x, axis=-1):
    return torch.softmax(x, dim=axis)
def _gelu(x):
    return torch.nn.functional.gelu(x, approximate="tanh")
def _silu(x):
    return torch.nn.functional.silu(x)
def _sigmoid(x):
    return torch.sigmoid(x)
def _layernorm(x, axis=-1, eps=1e-5):
    m = x.mean(dim=axis, keepdim=True)
    v = x.var(dim=axis, keepdim=True, unbiased=False)
    return (x - m) / torch.sqrt(v + eps)
def _scatter_add(tgt, idx, src, axis=0):
    return tgt.clone().index_add_(axis, idx, src)
def _segment_sum(src, seg, n):
    out = torch.zeros((n,) + src.shape[1:], dtype=src.dtype)
    return out.index_add_(0, seg, src)
def _pad(x, pw):
    flat = tuple(v for pair in reversed(pw) for v in pair)
    return torch.nn.functional.pad(x, flat)

kernel_name = 'standalone__cross_network_dcn__recsys'

def setup_inputs():

    _w = (lambda a: (lambda t: t.to(torch.bfloat16) if t.is_floating_point() else t)(torch.as_tensor(a)))
    B, F, D, L = 16384, 26, 32, 3
    Din = min(F * D, 1024)
    return {
        "x0": _w((lambda *s: torch.randn(*s, dtype=torch.bfloat16) * 0.05)(B, Din)),
        "ws": _w((lambda *s: torch.randn(*s, dtype=torch.bfloat16) * 0.4)(L, Din, Din) * (Din ** -0.5)),
        "bs": _w((lambda *s: torch.randn(*s, dtype=torch.bfloat16) * 3.2)(L, Din) * 0.01),
    }

def reference(x0, ws, bs):
    x = x0
    L = ws.shape[0]
    for l in range(L):
        proj = torch.einsum('bd,de->be', x, ws[l]) + bs[l]
        x = x0 * proj + x
    return x
```

```cpp
#include <hip/hip_runtime.h>
#include <hip/hip_bf16.h>
#include <stdint.h>

typedef short    bf16x8 __attribute__((ext_vector_type(8)));
typedef float    f32x4  __attribute__((ext_vector_type(4)));
typedef unsigned u32x4  __attribute__((ext_vector_type(4)));
typedef unsigned u32x2  __attribute__((ext_vector_type(2)));
typedef __bf16   bf16x2 __attribute__((ext_vector_type(2)));

constexpr int DIM    = 832;
constexpr int NLAYER = 3;
constexpr int ROWS   = 64;
constexpr int KS     = DIM / 32;
constexpr int CT     = 13;
constexpr int RT     = 4;
constexpr int WCOLS  = CT * 16;
constexpr int ROWB   = DIM * 2;
constexpr int LSTR   = ROWB + 16;
constexpr int LDS_X  = ROWS * LSTR;
constexpr int LDS_BIAS = NLAYER * DIM * 2;
constexpr int FRAGS_PER_WAVE = NLAYER * KS * CT;
constexpr int RING = 2 * CT;

__global__ __launch_bounds__(256) void dcn_pack_w(const unsigned short* __restrict__ W, u32x4* __restrict__ P) {
    const int t    = blockIdx.x * 256 + threadIdx.x;
    const int lane = t & 63;
    int f = t >> 6;
    const int ct = f % CT;  f /= CT;
    const int ks = f % KS;  f /= KS;
    const int layer = f % NLAYER;
    const int w = f / NLAYER;
    const unsigned short* src = W + (size_t)layer * DIM * DIM + (size_t)(ks * 32 + (lane >> 4) * 8) * DIM
                                  + w * WCOLS + ct * 16 + (lane & 15);
    unsigned v[8];
#pragma unroll
    for (int j = 0; j < 8; ++j) v[j] = src[j * DIM];
    u32x4 o;
    o[0] = v[0] | (v[1] << 16);
    o[1] = v[2] | (v[3] << 16);
    o[2] = v[4] | (v[5] << 16);
    o[3] = v[6] | (v[7] << 16);
    P[t] = o;
}

__device__ __forceinline__ float bf_lo(unsigned u) { return __uint_as_float(u << 16); }
__device__ __forceinline__ float bf_hi(unsigned u) { return __uint_as_float(u & 0xffff0000u); }
__device__ __forceinline__ unsigned pack_bf16(float a, float b) {
    bf16x2 v; v[0] = (__bf16)a; v[1] = (__bf16)b;
    return __builtin_bit_cast(unsigned, v);
}

__global__ __launch_bounds__(256, 1)
void dcn_fused(const char* __restrict__ P, const u32x4* __restrict__ x0g,
               const unsigned short* __restrict__ bs, char* __restrict__ out) {
    __shared__ __attribute__((aligned(16))) char lds[LDS_X + LDS_BIAS];
    char* ldsx = lds;
    unsigned short* ldsb = (unsigned short*)(lds + LDS_X);

    const int tid  = threadIdx.x;
    const int lane = tid & 63;
    const int wave = __builtin_amdgcn_readfirstlane(tid >> 6);
    const int lr   = lane & 15;
    const int lq   = lane >> 4;

    const char* wsrc = P + (size_t)wave * FRAGS_PER_WAVE * 1024 + lane * 16;
    bf16x8 wr[RING];
#pragma unroll
    for (int i = 0; i < RING; ++i) wr[i] = *(const bf16x8*)(wsrc + i * 1024);
    wsrc += RING * 1024;

    {
        const u32x4* src = x0g + (size_t)blockIdx.x * (ROWS * ROWB / 16);
        u32x4 tmp[26];
#pragma unroll
        for (int i = 0; i < 26; ++i) tmp[i] = src[i * 256 + tid];
#pragma unroll
        for (int i = 0; i < 26; ++i) {
            const int q = i * 256 + tid;
            const int row = q / 104, c = q - row * 104;
            *(u32x4*)(ldsx + row * LSTR + c * 16) = tmp[i];
        }
        for (int i = tid; i < LDS_BIAS / 16; i += 256)
            ((u32x4*)ldsb)[i] = ((const u32x4*)bs)[i];
    }
    __syncthreads();

    const char* xrd = ldsx + lr * LSTR + lq * 16;

#pragma unroll
    for (int layer = 0; layer < NLAYER; ++layer) {
        f32x4 acc[RT][CT];
        int tb = threadIdx.x;
        asm volatile("" : "+v"(tb));
        const int boff = LDS_X + (layer * DIM + wave * WCOLS + ((tb >> 4) & 3) * 4) * 2;
#pragma unroll
        for (int ct = 0; ct < CT; ++ct) {
            const u32x2 bb = *(const u32x2*)(lds + boff + ct * 32);
            f32x4 bv;
            bv[0] = bf_lo(bb[0]); bv[1] = bf_hi(bb[0]); bv[2] = bf_lo(bb[1]); bv[3] = bf_hi(bb[1]);
#pragma unroll
            for (int rt = 0; rt < RT; ++rt) acc[rt][ct] = bv;
        }

        bf16x8 xa[RT], xb[RT];
#pragma unroll
        for (int rt = 0; rt < RT; ++rt) xa[rt] = *(const bf16x8*)(xrd + rt * 16 * LSTR);

        for (int ks2 = 0; ks2 < KS / 2; ++ks2) {
            const int ks = ks2 * 2;
#pragma unroll
            for (int rt = 0; rt < RT; ++rt) xb[rt] = *(const bf16x8*)(xrd + rt * 16 * LSTR + (ks + 1) * 64);
            __builtin_amdgcn_sched_barrier(0);
#pragma unroll
            for (int ct = 0; ct < CT; ++ct) {
#pragma unroll
                for (int rt = 0; rt < RT; ++rt)
                    acc[rt][ct] = __builtin_amdgcn_mfma_f32_16x16x32_bf16(wr[ct], xa[rt], acc[rt][ct], 0, 0, 0);
                __builtin_amdgcn_sched_barrier(0);
                wr[ct] = *(const bf16x8*)(wsrc + ct * 1024);
                __builtin_amdgcn_sched_barrier(0);
            }
            {
                const int kn = ks + 2;
#pragma unroll
                for (int rt = 0; rt < RT; ++rt) xa[rt] = *(const bf16x8*)(xrd + rt * 16 * LSTR + kn * 64);
            }
            __builtin_amdgcn_sched_barrier(0);
#pragma unroll
            for (int ct = 0; ct < CT; ++ct) {
#pragma unroll
                for (int rt = 0; rt < RT; ++rt)
                    acc[rt][ct] = __builtin_amdgcn_mfma_f32_16x16x32_bf16(wr[CT + ct], xb[rt], acc[rt][ct], 0, 0, 0);
                __builtin_amdgcn_sched_barrier(0);
                wr[CT + ct] = *(const bf16x8*)(wsrc + (CT + ct) * 1024);
                __builtin_amdgcn_sched_barrier(0);
            }
            wsrc += RING * 1024;
        }

        int tz = threadIdx.x;
        asm volatile("" : "+v"(tz));
        const int   e_lr  = tz & 15;
        const int   e_col = wave * WCOLS + ((tz >> 4) & 3) * 4;
        const int   epoff = e_lr * LSTR + e_col * 2;
        const char* x0ep  = (const char*)x0g + ((size_t)blockIdx.x * ROWS + e_lr) * ROWB + e_col * 2;
        char*       outep = out + ((size_t)blockIdx.x * ROWS + e_lr) * ROWB + e_col * 2;
        u32x2 z[2][CT];
        if (layer > 0) {
#pragma unroll
            for (int ct = 0; ct < CT; ++ct) z[0][ct] = *(const u32x2*)(x0ep + ct * 32);
        }
        __syncthreads();
#pragma unroll
        for (int rt = 0; rt < RT; ++rt) {
            int eoff = epoff + rt * 16 * LSTR;
            asm volatile("" : "+v"(eoff));
            u32x2 xl[CT];
#pragma unroll
            for (int ct = 0; ct < CT; ++ct) xl[ct] = *(const u32x2*)(ldsx + eoff + ct * 32);
            if (layer > 0 && rt + 1 < RT) {
                const char* zp = x0ep + (rt + 1) * 16 * ROWB;
                asm volatile("" : "+v"(zp));
#pragma unroll
                for (int ct = 0; ct < CT; ++ct) z[(rt + 1) & 1][ct] = *(const u32x2*)(zp + ct * 32);
            }
            char* op = outep + rt * 16 * ROWB;
            asm volatile("" : "+v"(op));
#pragma unroll
            for (int ct = 0; ct < CT; ++ct) {
                const u32x2 zz = (layer == 0) ? xl[ct] : z[rt & 1][ct];
                const f32x4 a = acc[rt][ct];
                const float r0 = fmaf(bf_lo(zz[0]), a[0], bf_lo(xl[ct][0]));
                const float r1 = fmaf(bf_hi(zz[0]), a[1], bf_hi(xl[ct][0]));
                const float r2 = fmaf(bf_lo(zz[1]), a[2], bf_lo(xl[ct][1]));
                const float r3 = fmaf(bf_hi(zz[1]), a[3], bf_hi(xl[ct][1]));
                u32x2 o; o[0] = pack_bf16(r0, r1); o[1] = pack_bf16(r2, r3);
                if (layer < NLAYER - 1) *(u32x2*)(ldsx + eoff + ct * 32) = o;
                else *(u32x2*)(op + ct * 32) = o;
            }
            __builtin_amdgcn_sched_barrier(0);
        }
        if (layer < NLAYER - 1) __syncthreads();
    }
}

extern "C" void kernel_launch(void* const* d_in, const int* in_sizes, int n_in,
                              void* d_out, int out_size, void* d_ws, size_t ws_size,
                              hipStream_t stream) {
    const unsigned short* x0 = (const unsigned short*)d_in[0];
    const unsigned short* ws = (const unsigned short*)d_in[1];
    const unsigned short* bs = (const unsigned short*)d_in[2];
    const int B = in_sizes[0] / DIM;

    char* P = (char*)d_ws;
    const int pack_threads = 4 * FRAGS_PER_WAVE * 64;
    dcn_pack_w<<<pack_threads / 256, 256, 0, stream>>>(ws, (u32x4*)P);
    dcn_fused<<<B / ROWS, 256, 0, stream>>>(P, (const u32x4*)x0, bs, (char*)d_out);
}
```

```cpp
#include <hip/hip_runtime.h>
#include <hip/hip_bf16.h>
#include <stdint.h>

typedef unsigned u32x4 __attribute__((ext_vector_type(4)));

constexpr int DIM    = 832;
constexpr int NLAYER = 3;
constexpr int ROWS   = 64;
constexpr int KS     = DIM / 32;
constexpr int CT     = 13;
constexpr int WCOLS  = CT * 16;
constexpr int LSTR   = DIM * 2 + 16;
constexpr int FRAGS_PER_WAVE = NLAYER * KS * CT;

__global__ __launch_bounds__(256) void dcn_pack_w(const unsigned short* __restrict__ W, u32x4* __restrict__ P) {
    const int t    = blockIdx.x * 256 + threadIdx.x;
    const int lane = t & 63;
    int f = t >> 6;
    const int ct = f % CT;  f /= CT;
    const int ks = f % KS;  f /= KS;
    const int layer = f % NLAYER;
    const int w = f / NLAYER;
    const unsigned short* src = W + (size_t)layer * DIM * DIM + (size_t)(ks * 32 + (lane >> 4) * 8) * DIM
                                  + w * WCOLS + ct * 16 + (lane & 15);
    unsigned v[8];
#pragma unroll
    for (int j = 0; j < 8; ++j) v[j] = src[j * DIM];
    u32x4 o;
    o[0] = v[0] | (v[1] << 16);
    o[1] = v[2] | (v[3] << 16);
    o[2] = v[4] | (v[5] << 16);
    o[3] = v[6] | (v[7] << 16);
    P[t] = o;
}

__global__ __launch_bounds__(256, 1)
void dcn_fused(const char* __restrict__ P, const char* __restrict__ x0g,
               const char* __restrict__ bs, char* __restrict__ out) {
    __shared__ __attribute__((aligned(16))) char lds[ROWS * LSTR + 1024];
    const int wave = __builtin_amdgcn_readfirstlane(threadIdx.x >> 6);
    const char* pw = P + (size_t)wave * FRAGS_PER_WAVE * 1024;
    const char* px = x0g + (size_t)blockIdx.x * ROWS * DIM * 2;
    char*       po = out + (size_t)blockIdx.x * ROWS * DIM * 2;
    unsigned long long* dbg = (unsigned long long*)(P + (64u << 20)) + (blockIdx.x * 4 + wave) * 16; (void)dbg;
    *(volatile char*)&lds[ROWS * LSTR + threadIdx.x] = 0;
    asm volatile(
    "s_mov_b64 s[40:41], %0\n"
    "s_mov_b64 s[50:51], %1\n"
    "s_mov_b64 s[52:53], %2\n"
    "s_mov_b64 s[54:55], %3\n"
    "s_mov_b32 s56, %4\n"
    "v_mbcnt_lo_u32_b32 v240, -1, 0\n"
    "v_mbcnt_hi_u32_b32 v240, -1, v240\n"
    "v_lshlrev_b32 v241, 4, v240\n"
    "v_lshrrev_b32 v253, 4, v240\n"
    "v_lshlrev_b32 v253, 3, v253\n"
    "s_mul_i32 s58, s56, 416\n"
    "v_add_u32 v253, s58, v253\n"
    "global_load_dwordx2 v[104:105], v253, s[52:53] offset:0\n"
    "global_load_dwordx2 v[106:107], v253, s[52:53] offset:32\n"
    "global_load_dwordx2 v[108:109], v253, s[52:53] offset:64\n"
    "global_load_dwordx2 v[110:111], v253, s[52:53] offset:96\n"
    "global_load_dwordx2 v[112:113], v253, s[52:53] offset:128\n"
    "global_load_dwordx2 v[114:115], v253, s[52:53] offset:160\n"
    "global_load_dwordx2 v[116:117], v253, s[52:53] offset:192\n"
    "global_load_dwordx2 v[118:119], v253, s[52:53] offset:224\n"
    "global_load_dwordx2 v[120:121], v253, s[52:53] offset:256\n"
    "global_load_dwordx2 v[122:123], v253, s[52:53] offset:288\n"
    "global_load_dwordx2 v[124:125], v253, s[52:53] offset:320\n"
    "global_load_dwordx2 v[126:127], v253, s[52:53] offset:352\n"
    "global_load_dwordx2 v[128:129], v253, s[52:53] offset:384\n"
    "s_add_u32 s58, s40, 0\n"
    "s_addc_u32 s59, s41, 0\n"
    "global_load_dwordx4 v[0:3], v241, s[58:59] offset:0\n"
    "global_load_dwordx4 v[4:7], v241, s[58:59] offset:1024\n"
    "global_load_dwordx4 v[8:11], v241, s[58:59] offset:2048\n"
    "global_load_dwordx4 v[12:15], v241, s[58:59] offset:3072\n"
    "s_add_u32 s58, s40, 4096\n"
    "s_addc_u32 s59, s41, 0\n"
    "global_load_dwordx4 v[16:19], v241, s[58:59] offset:0\n"
    "global_load_dwordx4 v[20:23], v241, s[58:59] offset:1024\n"
    "global_load_dwordx4 v[24:27], v241, s[58:59] offset:2048\n"
    "global_load_dwordx4 v[28:31], v241, s[58:59] offset:3072\n"
    "s_add_u32 s58, s40, 8192\n"
    "s_addc_u32 s59, s41, 0\n"
    "global_load_dwordx4 v[32:35], v241, s[58:59] offset:0\n"
    "global_load_dwordx4 v[36:39], v241, s[58:59] offset:1024\n"
    "global_load_dwordx4 v[40:43], v241, s[58:59] offset:2048\n"
    "global_load_dwordx4 v[44:47], v241, s[58:59] offset:3072\n"
    "s_add_u32 s58, s40, 12288\n"
    "s_addc_u32 s59, s41, 0\n"
    "global_load_dwordx4 v[48:51], v241, s[58:59] offset:0\n"
    "global_load_dwordx4 v[52:55], v241, s[58:59] offset:1024\n"
    "global_load_dwordx4 v[56:59], v241, s[58:59] offset:2048\n"
    "global_load_dwordx4 v[60:63], v241, s[58:59] offset:3072\n"
    "s_add_u32 s58, s40, 16384\n"
    "s_addc_u32 s59, s41, 0\n"
    "global_load_dwordx4 v[64:67], v241, s[58:59] offset:0\n"
    "global_load_dwordx4 v[68:71], v241, s[58:59] offset:1024\n"
    "global_load_dwordx4 v[72:75], v241, s[58:59] offset:2048\n"
    "global_load_dwordx4 v[76:79], v241, s[58:59] offset:3072\n"
    "s_add_u32 s58, s40, 20480\n"
    "s_addc_u32 s59, s41, 0\n"
    "global_load_dwordx4 v[80:83], v241, s[58:59] offset:0\n"
    "global_load_dwordx4 v[84:87], v241, s[58:59] offset:1024\n"
    "global_load_dwordx4 v[88:91], v241, s[58:59] offset:2048\n"
    "global_load_dwordx4 v[92:95], v241, s[58:59] offset:3072\n"
    "s_add_u32 s58, s40, 24576\n"
    "s_addc_u32 s59, s41, 0\n"
    "global_load_dwordx4 v[96:99], v241, s[58:59] offset:0\n"
    "global_load_dwordx4 v[100:103], v241, s[58:59] offset:1024\n"
    "s_add_u32 s40, s40, 26624\n"
    "s_addc_u32 s41, s41, 0\n"
    "s_lshl_b32 s58, s56, 10\n"
    "v_add_u32 v244, s58, v241\n"
    "s_add_u32 s58, s50, 0\n"
    "s_addc_u32 s59, s51, 0\n"
    "global_load_dwordx4 v[136:139], v244, s[58:59]\n"
    "s_add_u32 s58, s50, 4096\n"
    "s_addc_u32 s59, s51, 0\n"
    "global_load_dwordx4 v[140:143], v244, s[58:59]\n"
    "s_add_u32 s58, s50, 8192\n"
    "s_addc_u32 s59, s51, 0\n"
    "global_load_dwordx4 v[144:147], v244, s[58:59]\n"
    "s_add_u32 s58, s50, 12288\n"
    "s_addc_u32 s59, s51, 0\n"
    "global_load_dwordx4 v[148:151], v244, s[58:59]\n"
    "s_add_u32 s58, s50, 16384\n"
    "s_addc_u32 s59, s51, 0\n"
    "global_load_dwordx4 v[152:155], v244, s[58:59]\n"
    "s_add_u32 s58, s50, 20480\n"
    "s_addc_u32 s59, s51, 0\n"
    "global_load_dwordx4 v[156:159], v244, s[58:59]\n"
    "s_add_u32 s58, s50, 24576\n"
    "s_addc_u32 s59, s51, 0\n"
    "global_load_dwordx4 v[160:163], v244, s[58:59]\n"
    "s_add_u32 s58, s50, 28672\n"
    "s_addc_u32 s59, s51, 0\n"
    "global_load_dwordx4 v[164:167], v244, s[58:59]\n"
    "s_add_u32 s58, s50, 32768\n"
    "s_addc_u32 s59, s51, 0\n"
    "global_load_dwordx4 v[168:171], v244, s[58:59]\n"
    "s_add_u32 s58, s50, 36864\n"
    "s_addc_u32 s59, s51, 0\n"
    "global_load_dwordx4 v[172:175], v244, s[58:59]\n"
    "s_add_u32 s58, s50, 40960\n"
    "s_addc_u32 s59, s51, 0\n"
    "global_load_dwordx4 v[176:179], v244, s[58:59]\n"
    "s_add_u32 s58, s50, 45056\n"
    "s_addc_u32 s59, s51, 0\n"
    "global_load_dwordx4 v[180:183], v244, s[58:59]\n"
    "s_add_u32 s58, s50, 49152\n"
    "s_addc_u32 s59, s51, 0\n"
    "global_load_dwordx4 v[184:187], v244, s[58:59]\n"
    "s_add_u32 s58, s50, 53248\n"
    "s_addc_u32 s59, s51, 0\n"
    "global_load_dwordx4 v[188:191], v244, s[58:59]\n"
    "s_add_u32 s58, s50, 57344\n"
    "s_addc_u32 s59, s51, 0\n"
    "global_load_dwordx4 v[192:195], v244, s[58:59]\n"
    "s_add_u32 s58, s50, 61440\n"
    "s_addc_u32 s59, s51, 0\n"
    "global_load_dwordx4 v[196:199], v244, s[58:59]\n"
    "s_add_u32 s58, s50, 65536\n"
    "s_addc_u32 s59, s51, 0\n"
    "global_load_dwordx4 v[200:203], v244, s[58:59]\n"
    "s_add_u32 s58, s50, 69632\n"
    "s_addc_u32 s59, s51, 0\n"
    "global_load_dwordx4 v[204:207], v244, s[58:59]\n"
    "s_add_u32 s58, s50, 73728\n"
    "s_addc_u32 s59, s51, 0\n"
    "global_load_dwordx4 v[208:211], v244, s[58:59]\n"
    "s_add_u32 s58, s50, 77824\n"
    "s_addc_u32 s59, s51, 0\n"
    "global_load_dwordx4 v[212:215], v244, s[58:59]\n"
    "s_add_u32 s58, s50, 81920\n"
    "s_addc_u32 s59, s51, 0\n"
    "global_load_dwordx4 v[216:219], v244, s[58:59]\n"
    "s_add_u32 s58, s50, 86016\n"
    "s_addc_u32 s59, s51, 0\n"
    "global_load_dwordx4 v[220:223], v244, s[58:59]\n"
    "s_add_u32 s58, s50, 90112\n"
    "s_addc_u32 s59, s51, 0\n"
    "global_load_dwordx4 v[224:227], v244, s[58:59]\n"
    "s_add_u32 s58, s50, 94208\n"
    "s_addc_u32 s59, s51, 0\n"
    "global_load_dwordx4 v[228:231], v244, s[58:59]\n"
    "s_add_u32 s58, s50, 98304\n"
    "s_addc_u32 s59, s51, 0\n"
    "global_load_dwordx4 v[232:235], v244, s[58:59]\n"
    "s_add_u32 s58, s50, 102400\n"
    "s_addc_u32 s59, s51, 0\n"
    "global_load_dwordx4 v[236:239], v244, s[58:59]\n"
    "v_lshrrev_b32 v245, 4, v244\n"
    "v_add_u32 v246, 0, v245\n"
    "v_mul_u32_u24 v247, 10083, v246\n"
    "v_lshrrev_b32 v247, 20, v247\n"
    "v_add_u32 v246, v246, v247\n"
    "v_lshlrev_b32 v246, 4, v246\n"
    "s_waitcnt vmcnt(25)\n"
    "ds_write_b128 v246, v[136:139]\n"
    "v_add_u32 v246, 256, v245\n"
    "v_mul_u32_u24 v247, 10083, v246\n"
    "v_lshrrev_b32 v247, 20, v247\n"
    "v_add_u32 v246, v246, v247\n"
    "v_lshlrev_b32 v246, 4, v246\n"
    "s_waitcnt vmcnt(24)\n"
    "ds_write_b128 v246, v[140:143]\n"
    "v_add_u32 v246, 512, v245\n"
    "v_mul_u32_u24 v247, 10083, v246\n"
    "v_lshrrev_b32 v247, 20, v247\n"
    "v_add_u32 v246, v246, v247\n"
    "v_lshlrev_b32 v246, 4, v246\n"
    "s_waitcnt vmcnt(23)\n"
    "ds_write_b128 v246, v[144:147]\n"
    "v_add_u32 v246, 768, v245\n"
    "v_mul_u32_u24 v247, 10083, v246\n"
    "v_lshrrev_b32 v247, 20, v247\n"
    "v_add_u32 v246, v246, v247\n"
    "v_lshlrev_b32 v246, 4, v246\n"
    "s_waitcnt vmcnt(22)\n"
    "ds_write_b128 v246, v[148:151]\n"
    "v_add_u32 v246, 1024, v245\n"
    "v_mul_u32_u24 v247, 10083, v246\n"
    "v_lshrrev_b32 v247, 20, v247\n"
    "v_add_u32 v246, v246, v247\n"
    "v_lshlrev_b32 v246, 4, v246\n"
    "s_waitcnt vmcnt(21)\n"
    "ds_write_b128 v246, v[152:155]\n"
    "v_add_u32 v246, 1280, v245\n"
    "v_mul_u32_u24 v247, 10083, v246\n"
    "v_lshrrev_b32 v247, 20, v247\n"
    "v_add_u32 v246, v246, v247\n"
    "v_lshlrev_b32 v246, 4, v246\n"
    "s_waitcnt vmcnt(20)\n"
    "ds_write_b128 v246, v[156:159]\n"
    "v_add_u32 v246, 1536, v245\n"
    "v_mul_u32_u24 v247, 10083, v246\n"
    "v_lshrrev_b32 v247, 20, v247\n"
    "v_add_u32 v246, v246, v247\n"
    "v_lshlrev_b32 v246, 4, v246\n"
    "s_waitcnt vmcnt(19)\n"
    "ds_write_b128 v246, v[160:163]\n"
    "v_add_u32 v246, 1792, v245\n"
    "v_mul_u32_u24 v247, 10083, v246\n"
    "v_lshrrev_b32 v247, 20, v247\n"
    "v_add_u32 v246, v246, v247\n"
    "v_lshlrev_b32 v246, 4, v246\n"
    "s_waitcnt vmcnt(18)\n"
    "ds_write_b128 v246, v[164:167]\n"
    "v_add_u32 v246, 2048, v245\n"
    "v_mul_u32_u24 v247, 10083, v246\n"
    "v_lshrrev_b32 v247, 20, v247\n"
    "v_add_u32 v246, v246, v247\n"
    "v_lshlrev_b32 v246, 4, v246\n"
    "s_waitcnt vmcnt(17)\n"
    "ds_write_b128 v246, v[168:171]\n"
    "v_add_u32 v246, 2304, v245\n"
    "v_mul_u32_u24 v247, 10083, v246\n"
    "v_lshrrev_b32 v247, 20, v247\n"
    "v_add_u32 v246, v246, v247\n"
    "v_lshlrev_b32 v246, 4, v246\n"
    "s_waitcnt vmcnt(16)\n"
    "ds_write_b128 v246, v[172:175]\n"
    "v_add_u32 v246, 2560, v245\n"
    "v_mul_u32_u24 v247, 10083, v246\n"
    "v_lshrrev_b32 v247, 20, v247\n"
    "v_add_u32 v246, v246, v247\n"
    "v_lshlrev_b32 v246, 4, v246\n"
    "s_waitcnt vmcnt(15)\n"
    "ds_write_b128 v246, v[176:179]\n"
    "v_add_u32 v246, 2816, v245\n"
    "v_mul_u32_u24 v247, 10083, v246\n"
    "v_lshrrev_b32 v247, 20, v247\n"
    "v_add_u32 v246, v246, v247\n"
    "v_lshlrev_b32 v246, 4, v246\n"
    "s_waitcnt vmcnt(14)\n"
    "ds_write_b128 v246, v[180:183]\n"
    "v_add_u32 v246, 3072, v245\n"
    "v_mul_u32_u24 v247, 10083, v246\n"
    "v_lshrrev_b32 v247, 20, v247\n"
    "v_add_u32 v246, v246, v247\n"
    "v_lshlrev_b32 v246, 4, v246\n"
    "s_waitcnt vmcnt(13)\n"
    "ds_write_b128 v246, v[184:187]\n"
    "v_add_u32 v246, 3328, v245\n"
    "v_mul_u32_u24 v247, 10083, v246\n"
    "v_lshrrev_b32 v247, 20, v247\n"
    "v_add_u32 v246, v246, v247\n"
    "v_lshlrev_b32 v246, 4, v246\n"
    "s_waitcnt vmcnt(12)\n"
    "ds_write_b128 v246, v[188:191]\n"
    "v_add_u32 v246, 3584, v245\n"
    "v_mul_u32_u24 v247, 10083, v246\n"
    "v_lshrrev_b32 v247, 20, v247\n"
    "v_add_u32 v246, v246, v247\n"
    "v_lshlrev_b32 v246, 4, v246\n"
    "s_waitcnt vmcnt(11)\n"
    "ds_write_b128 v246, v[192:195]\n"
    "v_add_u32 v246, 3840, v245\n"
    "v_mul_u32_u24 v247, 10083, v246\n"
    "v_lshrrev_b32 v247, 20, v247\n"
    "v_add_u32 v246, v246, v247\n"
    "v_lshlrev_b32 v246, 4, v246\n"
    "s_waitcnt vmcnt(10)\n"
    "ds_write_b128 v246, v[196:199]\n"
    "v_add_u32 v246, 4096, v245\n"
    "v_mul_u32_u24 v247, 10083, v246\n"
    "v_lshrrev_b32 v247, 20, v247\n"
    "v_add_u32 v246, v246, v247\n"
    "v_lshlrev_b32 v246, 4, v246\n"
    "s_waitcnt vmcnt(9)\n"
    "ds_write_b128 v246, v[200:203]\n"
    "v_add_u32 v246, 4352, v245\n"
    "v_mul_u32_u24 v247, 10083, v246\n"
    "v_lshrrev_b32 v247, 20, v247\n"
    "v_add_u32 v246, v246, v247\n"
    "v_lshlrev_b32 v246, 4, v246\n"
    "s_waitcnt vmcnt(8)\n"
    "ds_write_b128 v246, v[204:207]\n"
    "v_add_u32 v246, 4608, v245\n"
    "v_mul_u32_u24 v247, 10083, v246\n"
    "v_lshrrev_b32 v247, 20, v247\n"
    "v_add_u32 v246, v246, v247\n"
    "v_lshlrev_b32 v246, 4, v246\n"
    "s_waitcnt vmcnt(7)\n"
    "ds_write_b128 v246, v[208:211]\n"
    "v_add_u32 v246, 4864, v245\n"
    "v_mul_u32_u24 v247, 10083, v246\n"
    "v_lshrrev_b32 v247, 20, v247\n"
    "v_add_u32 v246, v246, v247\n"
    "v_lshlrev_b32 v246, 4, v246\n"
    "s_waitcnt vmcnt(6)\n"
    "ds_write_b128 v246, v[212:215]\n"
    "v_add_u32 v246, 5120, v245\n"
    "v_mul_u32_u24 v247, 10083, v246\n"
    "v_lshrrev_b32 v247, 20, v247\n"
    "v_add_u32 v246, v246, v247\n"
    "v_lshlrev_b32 v246, 4, v246\n"
    "s_waitcnt vmcnt(5)\n"
    "ds_write_b128 v246, v[216:219]\n"
    "v_add_u32 v246, 5376, v245\n"
    "v_mul_u32_u24 v247, 10083, v246\n"
    "v_lshrrev_b32 v247, 20, v247\n"
    "v_add_u32 v246, v246, v247\n"
    "v_lshlrev_b32 v246, 4, v246\n"
    "s_waitcnt vmcnt(4)\n"
    "ds_write_b128 v246, v[220:223]\n"
    "v_add_u32 v246, 5632, v245\n"
    "v_mul_u32_u24 v247, 10083, v246\n"
    "v_lshrrev_b32 v247, 20, v247\n"
    "v_add_u32 v246, v246, v247\n"
    "v_lshlrev_b32 v246, 4, v246\n"
    "s_waitcnt vmcnt(3)\n"
    "ds_write_b128 v246, v[224:227]\n"
    "v_add_u32 v246, 5888, v245\n"
    "v_mul_u32_u24 v247, 10083, v246\n"
    "v_lshrrev_b32 v247, 20, v247\n"
    "v_add_u32 v246, v246, v247\n"
    "v_lshlrev_b32 v246, 4, v246\n"
    "s_waitcnt vmcnt(2)\n"
    "ds_write_b128 v246, v[228:231]\n"
    "v_add_u32 v246, 6144, v245\n"
    "v_mul_u32_u24 v247, 10083, v246\n"
    "v_lshrrev_b32 v247, 20, v247\n"
    "v_add_u32 v246, v246, v247\n"
    "v_lshlrev_b32 v246, 4, v246\n"
    "s_waitcnt vmcnt(1)\n"
    "ds_write_b128 v246, v[232:235]\n"
    "v_add_u32 v246, 6400, v245\n"
    "v_mul_u32_u24 v247, 10083, v246\n"
    "v_lshrrev_b32 v247, 20, v247\n"
    "v_add_u32 v246, v246, v247\n"
    "v_lshlrev_b32 v246, 4, v246\n"
    "s_waitcnt vmcnt(0)\n"
    "ds_write_b128 v246, v[236:239]\n"
    "v_and_b32 v246, 15, v240\n"
    "v_mul_u32_u24 v242, 1680, v246\n"
    "v_lshrrev_b32 v247, 4, v240\n"
    "v_lshlrev_b32 v247, 4, v247\n"
    "v_add_u32 v242, v242, v247\n"
    "s_waitcnt lgkmcnt(0)\n"
    "s_barrier\n"
    "; ===================== layer 0 =====================\n"
    "s_waitcnt vmcnt(26)\n"
    "v_lshlrev_b32 v244, 16, v104\n"
    "v_and_b32 v245, 0xffff0000, v104\n"
    "v_lshlrev_b32 v246, 16, v105\n"
    "v_and_b32 v247, 0xffff0000, v105\n"
    "v_accvgpr_write_b32 a0, v244\n"
    "v_accvgpr_write_b32 a1, v245\n"
    "v_accvgpr_write_b32 a2, v246\n"
    "v_accvgpr_write_b32 a3, v247\n"
    "v_accvgpr_write_b32 a4, v244\n"
    "v_accvgpr_write_b32 a5, v245\n"
    "v_accvgpr_write_b32 a6, v246\n"
    "v_accvgpr_write_b32 a7, v247\n"
    "v_accvgpr_write_b32 a8, v244\n"
    "v_accvgpr_write_b32 a9, v245\n"
    "v_accvgpr_write_b32 a10, v246\n"
    "v_accvgpr_write_b32 a11, v247\n"
    "v_accvgpr_write_b32 a12, v244\n"
    "v_accvgpr_write_b32 a13, v245\n"
    "v_accvgpr_write_b32 a14, v246\n"
    "v_accvgpr_write_b32 a15, v247\n"
    "v_lshlrev_b32 v244, 16, v106\n"
    "v_and_b32 v245, 0xffff0000, v106\n"
    "v_lshlrev_b32 v246, 16, v107\n"
    "v_and_b32 v247, 0xffff0000, v107\n"
    "v_accvgpr_write_b32 a16, v244\n"
    "v_accvgpr_write_b32 a17, v245\n"
    "v_accvgpr_write_b32 a18, v246\n"
    "v_accvgpr_write_b32 a19, v247\n"
    "v_accvgpr_write_b32 a20, v244\n"
    "v_accvgpr_write_b32 a21, v245\n"
    "v_accvgpr_write_b32 a22, v246\n"
    "v_accvgpr_write_b32 a23, v247\n"
    "v_accvgpr_write_b32 a24, v244\n"
    "v_accvgpr_write_b32 a25, v245\n"
    "v_accvgpr_write_b32 a26, v246\n"
    "v_accvgpr_write_b32 a27, v247\n"
    "v_accvgpr_write_b32 a28, v244\n"
    "v_accvgpr_write_b32 a29, v245\n"
    "v_accvgpr_write_b32 a30, v246\n"
    "v_accvgpr_write_b32 a31, v247\n"
    "v_lshlrev_b32 v244, 16, v108\n"
    "v_and_b32 v245, 0xffff0000, v108\n"
    "v_lshlrev_b32 v246, 16, v109\n"
    "v_and_b32 v247, 0xffff0000, v109\n"
    "v_accvgpr_write_b32 a32, v244\n"
    "v_accvgpr_write_b32 a33, v245\n"
    "v_accvgpr_write_b32 a34, v246\n"
    "v_accvgpr_write_b32 a35, v247\n"
    "v_accvgpr_write_b32 a36, v244\n"
    "v_accvgpr_write_b32 a37, v245\n"
    "v_accvgpr_write_b32 a38, v246\n"
    "v_accvgpr_write_b32 a39, v247\n"
    "v_accvgpr_write_b32 a40, v244\n"
    "v_accvgpr_write_b32 a41, v245\n"
    "v_accvgpr_write_b32 a42, v246\n"
    "v_accvgpr_write_b32 a43, v247\n"
    "v_accvgpr_write_b32 a44, v244\n"
    "v_accvgpr_write_b32 a45, v245\n"
    "v_accvgpr_write_b32 a46, v246\n"
    "v_accvgpr_write_b32 a47, v247\n"
    "v_lshlrev_b32 v244, 16, v110\n"
    "v_and_b32 v245, 0xffff0000, v110\n"
    "v_lshlrev_b32 v246, 16, v111\n"
    "v_and_b32 v247, 0xffff0000, v111\n"
    "v_accvgpr_write_b32 a48, v244\n"
    "v_accvgpr_write_b32 a49, v245\n"
    "v_accvgpr_write_b32 a50, v246\n"
    "v_accvgpr_write_b32 a51, v247\n"
    "v_accvgpr_write_b32 a52, v244\n"
    "v_accvgpr_write_b32 a53, v245\n"
    "v_accvgpr_write_b32 a54, v246\n"
    "v_accvgpr_write_b32 a55, v247\n"
    "v_accvgpr_write_b32 a56, v244\n"
    "v_accvgpr_write_b32 a57, v245\n"
    "v_accvgpr_write_b32 a58, v246\n"
    "v_accvgpr_write_b32 a59, v247\n"
    "v_accvgpr_write_b32 a60, v244\n"
    "v_accvgpr_write_b32 a61, v245\n"
    "v_accvgpr_write_b32 a62, v246\n"
    "v_accvgpr_write_b32 a63, v247\n"
    "v_lshlrev_b32 v244, 16, v112\n"
    "v_and_b32 v245, 0xffff0000, v112\n"
    "v_lshlrev_b32 v246, 16, v113\n"
    "v_and_b32 v247, 0xffff0000, v113\n"
    "v_accvgpr_write_b32 a64, v244\n"
    "v_accvgpr_write_b32 a65, v245\n"
    "v_accvgpr_write_b32 a66, v246\n"
    "v_accvgpr_write_b32 a67, v247\n"
    "v_accvgpr_write_b32 a68, v244\n"
    "v_accvgpr_write_b32 a69, v245\n"
    "v_accvgpr_write_b32 a70, v246\n"
    "v_accvgpr_write_b32 a71, v247\n"
    "v_accvgpr_write_b32 a72, v244\n"
    "v_accvgpr_write_b32 a73, v245\n"
    "v_accvgpr_write_b32 a74, v246\n"
    "v_accvgpr_write_b32 a75, v247\n"
    "v_accvgpr_write_b32 a76, v244\n"
    "v_accvgpr_write_b32 a77, v245\n"
    "v_accvgpr_write_b32 a78, v246\n"
    "v_accvgpr_write_b32 a79, v247\n"
    "v_lshlrev_b32 v244, 16, v114\n"
    "v_and_b32 v245, 0xffff0000, v114\n"
    "v_lshlrev_b32 v246, 16, v115\n"
    "v_and_b32 v247, 0xffff0000, v115\n"
    "v_accvgpr_write_b32 a80, v244\n"
    "v_accvgpr_write_b32 a81, v245\n"
    "v_accvgpr_write_b32 a82, v246\n"
    "v_accvgpr_write_b32 a83, v247\n"
    "v_accvgpr_write_b32 a84, v244\n"
    "v_accvgpr_write_b32 a85, v245\n"
    "v_accvgpr_write_b32 a86, v246\n"
    "v_accvgpr_write_b32 a87, v247\n"
    "v_accvgpr_write_b32 a88, v244\n"
    "v_accvgpr_write_b32 a89, v245\n"
    "v_accvgpr_write_b32 a90, v246\n"
    "v_accvgpr_write_b32 a91, v247\n"
    "v_accvgpr_write_b32 a92, v244\n"
    "v_accvgpr_write_b32 a93, v245\n"
    "v_accvgpr_write_b32 a94, v246\n"
    "v_accvgpr_write_b32 a95, v247\n"
    "v_lshlrev_b32 v244, 16, v116\n"
    "v_and_b32 v245, 0xffff0000, v116\n"
    "v_lshlrev_b32 v246, 16, v117\n"
    "v_and_b32 v247, 0xffff0000, v117\n"
    "v_accvgpr_write_b32 a96, v244\n"
    "v_accvgpr_write_b32 a97, v245\n"
    "v_accvgpr_write_b32 a98, v246\n"
    "v_accvgpr_write_b32 a99, v247\n"
    "v_accvgpr_write_b32 a100, v244\n"
    "v_accvgpr_write_b32 a101, v245\n"
    "v_accvgpr_write_b32 a102, v246\n"
    "v_accvgpr_write_b32 a103, v247\n"
    "v_accvgpr_write_b32 a104, v244\n"
    "v_accvgpr_write_b32 a105, v245\n"
    "v_accvgpr_write_b32 a106, v246\n"
    "v_accvgpr_write_b32 a107, v247\n"
    "v_accvgpr_write_b32 a108, v244\n"
    "v_accvgpr_write_b32 a109, v245\n"
    "v_accvgpr_write_b32 a110, v246\n"
    "v_accvgpr_write_b32 a111, v247\n"
    "v_lshlrev_b32 v244, 16, v118\n"
    "v_and_b32 v245, 0xffff0000, v118\n"
    "v_lshlrev_b32 v246, 16, v119\n"
    "v_and_b32 v247, 0xffff0000, v119\n"
    "v_accvgpr_write_b32 a112, v244\n"
    "v_accvgpr_write_b32 a113, v245\n"
    "v_accvgpr_write_b32 a114, v246\n"
    "v_accvgpr_write_b32 a115, v247\n"
    "v_accvgpr_write_b32 a116, v244\n"
    "v_accvgpr_write_b32 a117, v245\n"
    "v_accvgpr_write_b32 a118, v246\n"
    "v_accvgpr_write_b32 a119, v247\n"
    "v_accvgpr_write_b32 a120, v244\n"
    "v_accvgpr_write_b32 a121, v245\n"
    "v_accvgpr_write_b32 a122, v246\n"
    "v_accvgpr_write_b32 a123, v247\n"
    "v_accvgpr_write_b32 a124, v244\n"
    "v_accvgpr_write_b32 a125, v245\n"
    "v_accvgpr_write_b32 a126, v246\n"
    "v_accvgpr_write_b32 a127, v247\n"
    "v_lshlrev_b32 v244, 16, v120\n"
    "v_and_b32 v245, 0xffff0000, v120\n"
    "v_lshlrev_b32 v246, 16, v121\n"
    "v_and_b32 v247, 0xffff0000, v121\n"
    "v_accvgpr_write_b32 a128, v244\n"
    "v_accvgpr_write_b32 a129, v245\n"
    "v_accvgpr_write_b32 a130, v246\n"
    "v_accvgpr_write_b32 a131, v247\n"
    "v_accvgpr_write_b32 a132, v244\n"
    "v_accvgpr_write_b32 a133, v245\n"
    "v_accvgpr_write_b32 a134, v246\n"
    "v_accvgpr_write_b32 a135, v247\n"
    "v_accvgpr_write_b32 a136, v244\n"
    "v_accvgpr_write_b32 a137, v245\n"
    "v_accvgpr_write_b32 a138, v246\n"
    "v_accvgpr_write_b32 a139, v247\n"
    "v_accvgpr_write_b32 a140, v244\n"
    "v_accvgpr_write_b32 a141, v245\n"
    "v_accvgpr_write_b32 a142, v246\n"
    "v_accvgpr_write_b32 a143, v247\n"
    "v_lshlrev_b32 v244, 16, v122\n"
    "v_and_b32 v245, 0xffff0000, v122\n"
    "v_lshlrev_b32 v246, 16, v123\n"
    "v_and_b32 v247, 0xffff0000, v123\n"
    "v_accvgpr_write_b32 a144, v244\n"
    "v_accvgpr_write_b32 a145, v245\n"
    "v_accvgpr_write_b32 a146, v246\n"
    "v_accvgpr_write_b32 a147, v247\n"
    "v_accvgpr_write_b32 a148, v244\n"
    "v_accvgpr_write_b32 a149, v245\n"
    "v_accvgpr_write_b32 a150, v246\n"
    "v_accvgpr_write_b32 a151, v247\n"
    "v_accvgpr_write_b32 a152, v244\n"
    "v_accvgpr_write_b32 a153, v245\n"
    "v_accvgpr_write_b32 a154, v246\n"
    "v_accvgpr_write_b32 a155, v247\n"
    "v_accvgpr_write_b32 a156, v244\n"
    "v_accvgpr_write_b32 a157, v245\n"
    "v_accvgpr_write_b32 a158, v246\n"
    "v_accvgpr_write_b32 a159, v247\n"
    "v_lshlrev_b32 v244, 16, v124\n"
    "v_and_b32 v245, 0xffff0000, v124\n"
    "v_lshlrev_b32 v246, 16, v125\n"
    "v_and_b32 v247, 0xffff0000, v125\n"
    "v_accvgpr_write_b32 a160, v244\n"
    "v_accvgpr_write_b32 a161, v245\n"
    "v_accvgpr_write_b32 a162, v246\n"
    "v_accvgpr_write_b32 a163, v247\n"
    "v_accvgpr_write_b32 a164, v244\n"
    "v_accvgpr_write_b32 a165, v245\n"
    "v_accvgpr_write_b32 a166, v246\n"
    "v_accvgpr_write_b32 a167, v247\n"
    "v_accvgpr_write_b32 a168, v244\n"
    "v_accvgpr_write_b32 a169, v245\n"
    "v_accvgpr_write_b32 a170, v246\n"
    "v_accvgpr_write_b32 a171, v247\n"
    "v_accvgpr_write_b32 a172, v244\n"
    "v_accvgpr_write_b32 a173, v245\n"
    "v_accvgpr_write_b32 a174, v246\n"
    "v_accvgpr_write_b32 a175, v247\n"
    "v_lshlrev_b32 v244, 16, v126\n"
    "v_and_b32 v245, 0xffff0000, v126\n"
    "v_lshlrev_b32 v246, 16, v127\n"
    "v_and_b32 v247, 0xffff0000, v127\n"
    "v_accvgpr_write_b32 a176, v244\n"
    "v_accvgpr_write_b32 a177, v245\n"
    "v_accvgpr_write_b32 a178, v246\n"
    "v_accvgpr_write_b32 a179, v247\n"
    "v_accvgpr_write_b32 a180, v244\n"
    "v_accvgpr_write_b32 a181, v245\n"
    "v_accvgpr_write_b32 a182, v246\n"
    "v_accvgpr_write_b32 a183, v247\n"
    "v_accvgpr_write_b32 a184, v244\n"
    "v_accvgpr_write_b32 a185, v245\n"
    "v_accvgpr_write_b32 a186, v246\n"
    "v_accvgpr_write_b32 a187, v247\n"
    "v_accvgpr_write_b32 a188, v244\n"
    "v_accvgpr_write_b32 a189, v245\n"
    "v_accvgpr_write_b32 a190, v246\n"
    "v_accvgpr_write_b32 a191, v247\n"
    "v_lshlrev_b32 v244, 16, v128\n"
    "v_and_b32 v245, 0xffff0000, v128\n"
    "v_lshlrev_b32 v246, 16, v129\n"
    "v_and_b32 v247, 0xffff0000, v129\n"
    "v_accvgpr_write_b32 a192, v244\n"
    "v_accvgpr_write_b32 a193, v245\n"
    "v_accvgpr_write_b32 a194, v246\n"
    "v_accvgpr_write_b32 a195, v247\n"
    "v_accvgpr_write_b32 a196, v244\n"
    "v_accvgpr_write_b32 a197, v245\n"
    "v_accvgpr_write_b32 a198, v246\n"
    "v_accvgpr_write_b32 a199, v247\n"
    "v_accvgpr_write_b32 a200, v244\n"
    "v_accvgpr_write_b32 a201, v245\n"
    "v_accvgpr_write_b32 a202, v246\n"
    "v_accvgpr_write_b32 a203, v247\n"
    "v_accvgpr_write_b32 a204, v244\n"
    "v_accvgpr_write_b32 a205, v245\n"
    "v_accvgpr_write_b32 a206, v246\n"
    "v_accvgpr_write_b32 a207, v247\n"
    "v_and_b32 v246, 15, v240\n"
    "v_mul_u32_u24 v242, 1680, v246\n"
    "v_lshrrev_b32 v247, 4, v240\n"
    "v_lshlrev_b32 v247, 4, v247\n"
    "v_add_u32 v242, v242, v247\n"
    "v_add_u32 v243, 53760, v242\n"
    "ds_read_b128 v[104:107], v242 offset:0\n"
    "ds_read_b128 v[108:111], v242 offset:26880\n"
    "ds_read_b128 v[112:115], v243 offset:0\n"
    "ds_read_b128 v[116:119], v243 offset:26880\n"
    "s_mov_b32 s57, 13\n"
    "L_loop_0_%=:\n"
    "s_add_u32 s42, s40, 4096\n"
    "s_addc_u32 s43, s41, 0\n"
    "s_add_u32 s44, s40, 12288\n"
    "s_addc_u32 s45, s41, 0\n"
    "s_add_u32 s46, s40, 20480\n"
    "s_addc_u32 s47, s41, 0\n"
    "s_add_u32 s48, s40, 28672\n"
    "s_addc_u32 s49, s41, 0\n"
    "s_add_u32 s40, s40, 26624\n"
    "s_addc_u32 s41, s41, 0\n"
    "ds_read_b128 v[120:123], v242 offset:64\n"
    "ds_read_b128 v[124:127], v242 offset:26944\n"
    "ds_read_b128 v[128:131], v243 offset:64\n"
    "ds_read_b128 v[132:135], v243 offset:26944\n"
    "s_waitcnt vmcnt(25) lgkmcnt(4)\n"
    "v_mfma_f32_16x16x32_bf16 a[0:3], v[0:3], v[104:107], a[0:3]\n"
    "v_mfma_f32_16x16x32_bf16 a[4:7], v[0:3], v[108:111], a[4:7]\n"
    "v_mfma_f32_16x16x32_bf16 a[8:11], v[0:3], v[112:115], a[8:11]\n"
    "v_mfma_f32_16x16x32_bf16 a[12:15], v[0:3], v[116:119], a[12:15]\n"
    "global_load_dwordx4 v[0:3], v241, s[42:43] offset:-4096\n"
    "s_waitcnt vmcnt(25)\n"
    "v_mfma_f32_16x16x32_bf16 a[16:19], v[4:7], v[104:107], a[16:19]\n"
    "v_mfma_f32_16x16x32_bf16 a[20:23], v[4:7], v[108:111], a[20:23]\n"
    "v_mfma_f32_16x16x32_bf16 a[24:27], v[4:7], v[112:115], a[24:27]\n"
    "v_mfma_f32_16x16x32_bf16 a[28:31], v[4:7], v[116:119], a[28:31]\n"
    "global_load_dwordx4 v[4:7], v241, s[42:43] offset:-3072\n"
    "s_waitcnt vmcnt(25)\n"
    "v_mfma_f32_16x16x32_bf16 a[32:35], v[8:11], v[104:107], a[32:35]\n"
    "v_mfma_f32_16x16x32_bf16 a[36:39], v[8:11], v[108:111], a[36:39]\n"
    "v_mfma_f32_16x16x32_bf16 a[40:43], v[8:11], v[112:115], a[40:43]\n"
    "v_mfma_f32_16x16x32_bf16 a[44:47], v[8:11], v[116:119], a[44:47]\n"
    "global_load_dwordx4 v[8:11], v241, s[42:43] offset:-2048\n"
    "s_waitcnt vmcnt(25)\n"
    "v_mfma_f32_16x16x32_bf16 a[48:51], v[12:15], v[104:107], a[48:51]\n"
    "v_mfma_f32_16x16x32_bf16 a[52:55], v[12:15], v[108:111], a[52:55]\n"
    "v_mfma_f32_16x16x32_bf16 a[56:59], v[12:15], v[112:115], a[56:59]\n"
    "v_mfma_f32_16x16x32_bf16 a[60:63], v[12:15], v[116:119], a[60:63]\n"
    "global_load_dwordx4 v[12:15], v241, s[42:43] offset:-1024\n"
    "s_waitcnt vmcnt(25)\n"
    "v_mfma_f32_16x16x32_bf16 a[64:67], v[16:19], v[104:107], a[64:67]\n"
    "v_mfma_f32_16x16x32_bf16 a[68:71], v[16:19], v[108:111], a[68:71]\n"
    "v_mfma_f32_16x16x32_bf16 a[72:75], v[16:19], v[112:115], a[72:75]\n"
    "v_mfma_f32_16x16x32_bf16 a[76:79], v[16:19], v[116:119], a[76:79]\n"
    "global_load_dwordx4 v[16:19], v241, s[42:43] offset:0\n"
    "s_waitcnt vmcnt(25)\n"
    "v_mfma_f32_16x16x32_bf16 a[80:83], v[20:23], v[104:107], a[80:83]\n"
    "v_mfma_f32_16x16x32_bf16 a[84:87], v[20:23], v[108:111], a[84:87]\n"
    "v_mfma_f32_16x16x32_bf16 a[88:91], v[20:23], v[112:115], a[88:91]\n"
    "v_mfma_f32_16x16x32_bf16 a[92:95], v[20:23], v[116:119], a[92:95]\n"
    "global_load_dwordx4 v[20:23], v241, s[42:43] offset:1024\n"
    "s_waitcnt vmcnt(25)\n"
    "v_mfma_f32_16x16x32_bf16 a[96:99], v[24:27], v[104:107], a[96:99]\n"
    "v_mfma_f32_16x16x32_bf16 a[100:103], v[24:27], v[108:111], a[100:103]\n"
    "v_mfma_f32_16x16x32_bf16 a[104:107], v[24:27], v[112:115], a[104:107]\n"
    "v_mfma_f32_16x16x32_bf16 a[108:111], v[24:27], v[116:119], a[108:111]\n"
    "global_load_dwordx4 v[24:27], v241, s[42:43] offset:2048\n"
    "s_waitcnt vmcnt(25)\n"
    "v_mfma_f32_16x16x32_bf16 a[112:115], v[28:31], v[104:107], a[112:115]\n"
    "v_mfma_f32_16x16x32_bf16 a[116:119], v[28:31], v[108:111], a[116:119]\n"
    "v_mfma_f32_16x16x32_bf16 a[120:123], v[28:31], v[112:115], a[120:123]\n"
    "v_mfma_f32_16x16x32_bf16 a[124:127], v[28:31], v[116:119], a[124:127]\n"
    "global_load_dwordx4 v[28:31], v241, s[42:43] offset:3072\n"
    "s_waitcnt vmcnt(25)\n"
    "v_mfma_f32_16x16x32_bf16 a[128:131], v[32:35], v[104:107], a[128:131]\n"
    "v_mfma_f32_16x16x32_bf16 a[132:135], v[32:35], v[108:111], a[132:135]\n"
    "v_mfma_f32_16x16x32_bf16 a[136:139], v[32:35], v[112:115], a[136:139]\n"
    "v_mfma_f32_16x16x32_bf16 a[140:143], v[32:35], v[116:119], a[140:143]\n"
    "global_load_dwordx4 v[32:35], v241, s[44:45] offset:-4096\n"
    "s_waitcnt vmcnt(25)\n"
    "v_mfma_f32_16x16x32_bf16 a[144:147], v[36:39], v[104:107], a[144:147]\n"
    "v_mfma_f32_16x16x32_bf16 a[148:151], v[36:39], v[108:111], a[148:151]\n"
    "v_mfma_f32_16x16x32_bf16 a[152:155], v[36:39], v[112:115], a[152:155]\n"
    "v_mfma_f32_16x16x32_bf16 a[156:159], v[36:39], v[116:119], a[156:159]\n"
    "global_load_dwordx4 v[36:39], v241, s[44:45] offset:-3072\n"
    "s_waitcnt vmcnt(25)\n"
    "v_mfma_f32_16x16x32_bf16 a[160:163], v[40:43], v[104:107], a[160:163]\n"
    "v_mfma_f32_16x16x32_bf16 a[164:167], v[40:43], v[108:111], a[164:167]\n"
    "v_mfma_f32_16x16x32_bf16 a[168:171], v[40:43], v[112:115], a[168:171]\n"
    "v_mfma_f32_16x16x32_bf16 a[172:175], v[40:43], v[116:119], a[172:175]\n"
    "global_load_dwordx4 v[40:43], v241, s[44:45] offset:-2048\n"
    "s_waitcnt vmcnt(25)\n"
    "v_mfma_f32_16x16x32_bf16 a[176:179], v[44:47], v[104:107], a[176:179]\n"
    "v_mfma_f32_16x16x32_bf16 a[180:183], v[44:47], v[108:111], a[180:183]\n"
    "v_mfma_f32_16x16x32_bf16 a[184:187], v[44:47], v[112:115], a[184:187]\n"
    "v_mfma_f32_16x16x32_bf16 a[188:191], v[44:47], v[116:119], a[188:191]\n"
    "global_load_dwordx4 v[44:47], v241, s[44:45] offset:-1024\n"
    "s_waitcnt vmcnt(25)\n"
    "v_mfma_f32_16x16x32_bf16 a[192:195], v[48:51], v[104:107], a[192:195]\n"
    "v_mfma_f32_16x16x32_bf16 a[196:199], v[48:51], v[108:111], a[196:199]\n"
    "v_mfma_f32_16x16x32_bf16 a[200:203], v[48:51], v[112:115], a[200:203]\n"
    "v_mfma_f32_16x16x32_bf16 a[204:207], v[48:51], v[116:119], a[204:207]\n"
    "global_load_dwordx4 v[48:51], v241, s[44:45] offset:0\n"
    "ds_read_b128 v[104:107], v242 offset:128\n"
    "ds_read_b128 v[108:111], v242 offset:27008\n"
    "ds_read_b128 v[112:115], v243 offset:128\n"
    "ds_read_b128 v[116:119], v243 offset:27008\n"
    "s_waitcnt vmcnt(25) lgkmcnt(4)\n"
    "v_mfma_f32_16x16x32_bf16 a[0:3], v[52:55], v[120:123], a[0:3]\n"
    "v_mfma_f32_16x16x32_bf16 a[4:7], v[52:55], v[124:127], a[4:7]\n"
    "v_mfma_f32_16x16x32_bf16 a[8:11], v[52:55], v[128:131], a[8:11]\n"
    "v_mfma_f32_16x16x32_bf16 a[12:15], v[52:55], v[132:135], a[12:15]\n"
    "global_load_dwordx4 v[52:55], v241, s[44:45] offset:1024\n"
    "s_waitcnt vmcnt(25)\n"
    "v_mfma_f32_16x16x32_bf16 a[16:19], v[56:59], v[120:123], a[16:19]\n"
    "v_mfma_f32_16x16x32_bf16 a[20:23], v[56:59], v[124:127], a[20:23]\n"
    "v_mfma_f32_16x16x32_bf16 a[24:27], v[56:59], v[128:131], a[24:27]\n"
    "v_mfma_f32_16x16x32_bf16 a[28:31], v[56:59], v[132:135], a[28:31]\n"
    "global_load_dwordx4 v[56:59], v241, s[44:45] offset:2048\n"
    "s_waitcnt vmcnt(25)\n"
    "v_mfma_f32_16x16x32_bf16 a[32:35], v[60:63], v[120:123], a[32:35]\n"
    "v_mfma_f32_16x16x32_bf16 a[36:39], v[60:63], v[124:127], a[36:39]\n"
    "v_mfma_f32_16x16x32_bf16 a[40:43], v[60:63], v[128:131], a[40:43]\n"
    "v_mfma_f32_16x16x32_bf16 a[44:47], v[60:63], v[132:135], a[44:47]\n"
    "global_load_dwordx4 v[60:63], v241, s[44:45] offset:3072\n"
    "s_waitcnt vmcnt(25)\n"
    "v_mfma_f32_16x16x32_bf16 a[48:51], v[64:67], v[120:123], a[48:51]\n"
    "v_mfma_f32_16x16x32_bf16 a[52:55], v[64:67], v[124:127], a[52:55]\n"
    "v_mfma_f32_16x16x32_bf16 a[56:59], v[64:67], v[128:131], a[56:59]\n"
    "v_mfma_f32_16x16x32_bf16 a[60:63], v[64:67], v[132:135], a[60:63]\n"
    "global_load_dwordx4 v[64:67], v241, s[46:47] offset:-4096\n"
    "s_waitcnt vmcnt(25)\n"
    "v_mfma_f32_16x16x32_bf16 a[64:67], v[68:71], v[120:123], a[64:67]\n"
    "v_mfma_f32_16x16x32_bf16 a[68:71], v[68:71], v[124:127], a[68:71]\n"
    "v_mfma_f32_16x16x32_bf16 a[72:75], v[68:71], v[128:131], a[72:75]\n"
    "v_mfma_f32_16x16x32_bf16 a[76:79], v[68:71], v[132:135], a[76:79]\n"
    "global_load_dwordx4 v[68:71], v241, s[46:47] offset:-3072\n"
    "s_waitcnt vmcnt(25)\n"
    "v_mfma_f32_16x16x32_bf16 a[80:83], v[72:75], v[120:123], a[80:83]\n"
    "v_mfma_f32_16x16x32_bf16 a[84:87], v[72:75], v[124:127], a[84:87]\n"
    "v_mfma_f32_16x16x32_bf16 a[88:91], v[72:75], v[128:131], a[88:91]\n"
    "v_mfma_f32_16x16x32_bf16 a[92:95], v[72:75], v[132:135], a[92:95]\n"
    "global_load_dwordx4 v[72:75], v241, s[46:47] offset:-2048\n"
    "s_waitcnt vmcnt(25)\n"
    "v_mfma_f32_16x16x32_bf16 a[96:99], v[76:79], v[120:123], a[96:99]\n"
    "v_mfma_f32_16x16x32_bf16 a[100:103], v[76:79], v[124:127], a[100:103]\n"
    "v_mfma_f32_16x16x32_bf16 a[104:107], v[76:79], v[128:131], a[104:107]\n"
    "v_mfma_f32_16x16x32_bf16 a[108:111], v[76:79], v[132:135], a[108:111]\n"
    "global_load_dwordx4 v[76:79], v241, s[46:47] offset:-1024\n"
    "s_waitcnt vmcnt(25)\n"
    "v_mfma_f32_16x16x32_bf16 a[112:115], v[80:83], v[120:123], a[112:115]\n"
    "v_mfma_f32_16x16x32_bf16 a[116:119], v[80:83], v[124:127], a[116:119]\n"
    "v_mfma_f32_16x16x32_bf16 a[120:123], v[80:83], v[128:131], a[120:123]\n"
    "v_mfma_f32_16x16x32_bf16 a[124:127], v[80:83], v[132:135], a[124:127]\n"
    "global_load_dwordx4 v[80:83], v241, s[46:47] offset:0\n"
    "s_waitcnt vmcnt(25)\n"
    "v_mfma_f32_16x16x32_bf16 a[128:131], v[84:87], v[120:123], a[128:131]\n"
    "v_mfma_f32_16x16x32_bf16 a[132:135], v[84:87], v[124:127], a[132:135]\n"
    "v_mfma_f32_16x16x32_bf16 a[136:139], v[84:87], v[128:131], a[136:139]\n"
    "v_mfma_f32_16x16x32_bf16 a[140:143], v[84:87], v[132:135], a[140:143]\n"
    "global_load_dwordx4 v[84:87], v241, s[46:47] offset:1024\n"
    "s_waitcnt vmcnt(25)\n"
    "v_mfma_f32_16x16x32_bf16 a[144:147], v[88:91], v[120:123], a[144:147]\n"
    "v_mfma_f32_16x16x32_bf16 a[148:151], v[88:91], v[124:127], a[148:151]\n"
    "v_mfma_f32_16x16x32_bf16 a[152:155], v[88:91], v[128:131], a[152:155]\n"
    "v_mfma_f32_16x16x32_bf16 a[156:159], v[88:91], v[132:135], a[156:159]\n"
    "global_load_dwordx4 v[88:91], v241, s[46:47] offset:2048\n"
    "s_waitcnt vmcnt(25)\n"
    "v_mfma_f32_16x16x32_bf16 a[160:163], v[92:95], v[120:123], a[160:163]\n"
    "v_mfma_f32_16x16x32_bf16 a[164:167], v[92:95], v[124:127], a[164:167]\n"
    "v_mfma_f32_16x16x32_bf16 a[168:171], v[92:95], v[128:131], a[168:171]\n"
    "v_mfma_f32_16x16x32_bf16 a[172:175], v[92:95], v[132:135], a[172:175]\n"
    "global_load_dwordx4 v[92:95], v241, s[46:47] offset:3072\n"
    "s_waitcnt vmcnt(25)\n"
    "v_mfma_f32_16x16x32_bf16 a[176:179], v[96:99], v[120:123], a[176:179]\n"
    "v_mfma_f32_16x16x32_bf16 a[180:183], v[96:99], v[124:127], a[180:183]\n"
    "v_mfma_f32_16x16x32_bf16 a[184:187], v[96:99], v[128:131], a[184:187]\n"
    "v_mfma_f32_16x16x32_bf16 a[188:191], v[96:99], v[132:135], a[188:191]\n"
    "global_load_dwordx4 v[96:99], v241, s[48:49] offset:-4096\n"
    "s_waitcnt vmcnt(25)\n"
    "v_mfma_f32_16x16x32_bf16 a[192:195], v[100:103], v[120:123], a[192:195]\n"
    "v_mfma_f32_16x16x32_bf16 a[196:199], v[100:103], v[124:127], a[196:199]\n"
    "v_mfma_f32_16x16x32_bf16 a[200:203], v[100:103], v[128:131], a[200:203]\n"
    "v_mfma_f32_16x16x32_bf16 a[204:207], v[100:103], v[132:135], a[204:207]\n"
    "global_load_dwordx4 v[100:103], v241, s[48:49] offset:-3072\n"
    "v_add_u32 v242, 128, v242\n"
    "v_add_u32 v243, 128, v243\n"
    "s_sub_u32 s57, s57, 1\n"
    "s_cmp_lg_u32 s57, 0\n"
    "s_cbranch_scc1 L_loop_0_%=\n"
    "s_waitcnt lgkmcnt(0)\n"
    "s_nop 7\n"
    "s_nop 7\n"
    "s_barrier\n"
    "v_lshrrev_b32 v253, 4, v240\n"
    "v_lshlrev_b32 v253, 3, v253\n"
    "s_mul_i32 s58, s56, 416\n"
    "v_add_u32 v253, s58, v253\n"
    "v_and_b32 v254, 15, v240\n"
    "v_mul_u32_u24 v254, 1680, v254\n"
    "v_add_u32 v254, v254, v253\n"
    "v_add_u32 v255, 53760, v254\n"
    "ds_read_b64 v[136:137], v254 offset:0\n"
    "ds_read_b64 v[138:139], v254 offset:32\n"
    "ds_read_b64 v[140:141], v254 offset:64\n"
    "ds_read_b64 v[142:143], v254 offset:96\n"
    "ds_read_b64 v[144:145], v254 offset:128\n"
    "ds_read_b64 v[146:147], v254 offset:160\n"
    "ds_read_b64 v[148:149], v254 offset:192\n"
    "ds_read_b64 v[150:151], v254 offset:224\n"
    "ds_read_b64 v[152:153], v254 offset:256\n"
    "ds_read_b64 v[154:155], v254 offset:288\n"
    "ds_read_b64 v[156:157], v254 offset:320\n"
    "ds_read_b64 v[158:159], v254 offset:352\n"
    "ds_read_b64 v[160:161], v254 offset:384\n"
    "s_waitcnt lgkmcnt(12)\n"
    "v_accvgpr_read_b32 v244, a0\n"
    "v_accvgpr_read_b32 v245, a1\n"
    "v_accvgpr_read_b32 v246, a2\n"
    "v_accvgpr_read_b32 v247, a3\n"
    "v_lshlrev_b32 v248, 16, v136\n"
    "v_lshlrev_b32 v249, 16, v136\n"
    "v_fmac_f32 v249, v248, v244\n"
    "v_and_b32 v248, 0xffff0000, v136\n"
    "v_and_b32 v250, 0xffff0000, v136\n"
    "v_fmac_f32 v250, v248, v245\n"
    "v_cvt_pk_bf16_f32 v104, v249, v250\n"
    "v_lshlrev_b32 v248, 16, v137\n"
    "v_lshlrev_b32 v249, 16, v137\n"
    "v_fmac_f32 v249, v248, v246\n"
    "v_and_b32 v248, 0xffff0000, v137\n"
    "v_and_b32 v250, 0xffff0000, v137\n"
    "v_fmac_f32 v250, v248, v247\n"
    "v_cvt_pk_bf16_f32 v105, v249, v250\n"
    "s_waitcnt lgkmcnt(11)\n"
    "v_accvgpr_read_b32 v244, a16\n"
    "v_accvgpr_read_b32 v245, a17\n"
    "v_accvgpr_read_b32 v246, a18\n"
    "v_accvgpr_read_b32 v247, a19\n"
    "v_lshlrev_b32 v248, 16, v138\n"
    "v_lshlrev_b32 v249, 16, v138\n"
    "v_fmac_f32 v249, v248, v244\n"
    "v_and_b32 v248, 0xffff0000, v138\n"
    "v_and_b32 v250, 0xffff0000, v138\n"
    "v_fmac_f32 v250, v248, v245\n"
    "v_cvt_pk_bf16_f32 v106, v249, v250\n"
    "v_lshlrev_b32 v248, 16, v139\n"
    "v_lshlrev_b32 v249, 16, v139\n"
    "v_fmac_f32 v249, v248, v246\n"
    "v_and_b32 v248, 0xffff0000, v139\n"
    "v_and_b32 v250, 0xffff0000, v139\n"
    "v_fmac_f32 v250, v248, v247\n"
    "v_cvt_pk_bf16_f32 v107, v249, v250\n"
    "s_waitcnt lgkmcnt(10)\n"
    "v_accvgpr_read_b32 v244, a32\n"
    "v_accvgpr_read_b32 v245, a33\n"
    "v_accvgpr_read_b32 v246, a34\n"
    "v_accvgpr_read_b32 v247, a35\n"
    "v_lshlrev_b32 v248, 16, v140\n"
    "v_lshlrev_b32 v249, 16, v140\n"
    "v_fmac_f32 v249, v248, v244\n"
    "v_and_b32 v248, 0xffff0000, v140\n"
    "v_and_b32 v250, 0xffff0000, v140\n"
    "v_fmac_f32 v250, v248, v245\n"
    "v_cvt_pk_bf16_f32 v108, v249, v250\n"
    "v_lshlrev_b32 v248, 16, v141\n"
    "v_lshlrev_b32 v249, 16, v141\n"
    "v_fmac_f32 v249, v248, v246\n"
    "v_and_b32 v248, 0xffff0000, v141\n"
    "v_and_b32 v250, 0xffff0000, v141\n"
    "v_fmac_f32 v250, v248, v247\n"
    "v_cvt_pk_bf16_f32 v109, v249, v250\n"
    "s_waitcnt lgkmcnt(9)\n"
    "v_accvgpr_read_b32 v244, a48\n"
    "v_accvgpr_read_b32 v245, a49\n"
    "v_accvgpr_read_b32 v246, a50\n"
    "v_accvgpr_read_b32 v247, a51\n"
    "v_lshlrev_b32 v248, 16, v142\n"
    "v_lshlrev_b32 v249, 16, v142\n"
    "v_fmac_f32 v249, v248, v244\n"
    "v_and_b32 v248, 0xffff0000, v142\n"
    "v_and_b32 v250, 0xffff0000, v142\n"
    "v_fmac_f32 v250, v248, v245\n"
    "v_cvt_pk_bf16_f32 v110, v249, v250\n"
    "v_lshlrev_b32 v248, 16, v143\n"
    "v_lshlrev_b32 v249, 16, v143\n"
    "v_fmac_f32 v249, v248, v246\n"
    "v_and_b32 v248, 0xffff0000, v143\n"
    "v_and_b32 v250, 0xffff0000, v143\n"
    "v_fmac_f32 v250, v248, v247\n"
    "v_cvt_pk_bf16_f32 v111, v249, v250\n"
    "s_waitcnt lgkmcnt(8)\n"
    "v_accvgpr_read_b32 v244, a64\n"
    "v_accvgpr_read_b32 v245, a65\n"
    "v_accvgpr_read_b32 v246, a66\n"
    "v_accvgpr_read_b32 v247, a67\n"
    "v_lshlrev_b32 v248, 16, v144\n"
    "v_lshlrev_b32 v249, 16, v144\n"
    "v_fmac_f32 v249, v248, v244\n"
    "v_and_b32 v248, 0xffff0000, v144\n"
    "v_and_b32 v250, 0xffff0000, v144\n"
    "v_fmac_f32 v250, v248, v245\n"
    "v_cvt_pk_bf16_f32 v112, v249, v250\n"
    "v_lshlrev_b32 v248, 16, v145\n"
    "v_lshlrev_b32 v249, 16, v145\n"
    "v_fmac_f32 v249, v248, v246\n"
    "v_and_b32 v248, 0xffff0000, v145\n"
    "v_and_b32 v250, 0xffff0000, v145\n"
    "v_fmac_f32 v250, v248, v247\n"
    "v_cvt_pk_bf16_f32 v113, v249, v250\n"
    "s_waitcnt lgkmcnt(7)\n"
    "v_accvgpr_read_b32 v244, a80\n"
    "v_accvgpr_read_b32 v245, a81\n"
    "v_accvgpr_read_b32 v246, a82\n"
    "v_accvgpr_read_b32 v247, a83\n"
    "v_lshlrev_b32 v248, 16, v146\n"
    "v_lshlrev_b32 v249, 16, v146\n"
    "v_fmac_f32 v249, v248, v244\n"
    "v_and_b32 v248, 0xffff0000, v146\n"
    "v_and_b32 v250, 0xffff0000, v146\n"
    "v_fmac_f32 v250, v248, v245\n"
    "v_cvt_pk_bf16_f32 v114, v249, v250\n"
    "v_lshlrev_b32 v248, 16, v147\n"
    "v_lshlrev_b32 v249, 16, v147\n"
    "v_fmac_f32 v249, v248, v246\n"
    "v_and_b32 v248, 0xffff0000, v147\n"
    "v_and_b32 v250, 0xffff0000, v147\n"
    "v_fmac_f32 v250, v248, v247\n"
    "v_cvt_pk_bf16_f32 v115, v249, v250\n"
    "s_waitcnt lgkmcnt(6)\n"
    "v_accvgpr_read_b32 v244, a96\n"
    "v_accvgpr_read_b32 v245, a97\n"
    "v_accvgpr_read_b32 v246, a98\n"
    "v_accvgpr_read_b32 v247, a99\n"
    "v_lshlrev_b32 v248, 16, v148\n"
    "v_lshlrev_b32 v249, 16, v148\n"
    "v_fmac_f32 v249, v248, v244\n"
    "v_and_b32 v248, 0xffff0000, v148\n"
    "v_and_b32 v250, 0xffff0000, v148\n"
    "v_fmac_f32 v250, v248, v245\n"
    "v_cvt_pk_bf16_f32 v116, v249, v250\n"
    "v_lshlrev_b32 v248, 16, v149\n"
    "v_lshlrev_b32 v249, 16, v149\n"
    "v_fmac_f32 v249, v248, v246\n"
    "v_and_b32 v248, 0xffff0000, v149\n"
    "v_and_b32 v250, 0xffff0000, v149\n"
    "v_fmac_f32 v250, v248, v247\n"
    "v_cvt_pk_bf16_f32 v117, v249, v250\n"
    "s_waitcnt lgkmcnt(5)\n"
    "v_accvgpr_read_b32 v244, a112\n"
    "v_accvgpr_read_b32 v245, a113\n"
    "v_accvgpr_read_b32 v246, a114\n"
    "v_accvgpr_read_b32 v247, a115\n"
    "v_lshlrev_b32 v248, 16, v150\n"
    "v_lshlrev_b32 v249, 16, v150\n"
    "v_fmac_f32 v249, v248, v244\n"
    "v_and_b32 v248, 0xffff0000, v150\n"
    "v_and_b32 v250, 0xffff0000, v150\n"
    "v_fmac_f32 v250, v248, v245\n"
    "v_cvt_pk_bf16_f32 v118, v249, v250\n"
    "v_lshlrev_b32 v248, 16, v151\n"
    "v_lshlrev_b32 v249, 16, v151\n"
    "v_fmac_f32 v249, v248, v246\n"
    "v_and_b32 v248, 0xffff0000, v151\n"
    "v_and_b32 v250, 0xffff0000, v151\n"
    "v_fmac_f32 v250, v248, v247\n"
    "v_cvt_pk_bf16_f32 v119, v249, v250\n"
    "s_waitcnt lgkmcnt(4)\n"
    "v_accvgpr_read_b32 v244, a128\n"
    "v_accvgpr_read_b32 v245, a129\n"
    "v_accvgpr_read_b32 v246, a130\n"
    "v_accvgpr_read_b32 v247, a131\n"
    "v_lshlrev_b32 v248, 16, v152\n"
    "v_lshlrev_b32 v249, 16, v152\n"
    "v_fmac_f32 v249, v248, v244\n"
    "v_and_b32 v248, 0xffff0000, v152\n"
    "v_and_b32 v250, 0xffff0000, v152\n"
    "v_fmac_f32 v250, v248, v245\n"
    "v_cvt_pk_bf16_f32 v120, v249, v250\n"
    "v_lshlrev_b32 v248, 16, v153\n"
    "v_lshlrev_b32 v249, 16, v153\n"
    "v_fmac_f32 v249, v248, v246\n"
    "v_and_b32 v248, 0xffff0000, v153\n"
    "v_and_b32 v250, 0xffff0000, v153\n"
    "v_fmac_f32 v250, v248, v247\n"
    "v_cvt_pk_bf16_f32 v121, v249, v250\n"
    "s_waitcnt lgkmcnt(3)\n"
    "v_accvgpr_read_b32 v244, a144\n"
    "v_accvgpr_read_b32 v245, a145\n"
    "v_accvgpr_read_b32 v246, a146\n"
    "v_accvgpr_read_b32 v247, a147\n"
    "v_lshlrev_b32 v248, 16, v154\n"
    "v_lshlrev_b32 v249, 16, v154\n"
    "v_fmac_f32 v249, v248, v244\n"
    "v_and_b32 v248, 0xffff0000, v154\n"
    "v_and_b32 v250, 0xffff0000, v154\n"
    "v_fmac_f32 v250, v248, v245\n"
    "v_cvt_pk_bf16_f32 v122, v249, v250\n"
    "v_lshlrev_b32 v248, 16, v155\n"
    "v_lshlrev_b32 v249, 16, v155\n"
    "v_fmac_f32 v249, v248, v246\n"
    "v_and_b32 v248, 0xffff0000, v155\n"
    "v_and_b32 v250, 0xffff0000, v155\n"
    "v_fmac_f32 v250, v248, v247\n"
    "v_cvt_pk_bf16_f32 v123, v249, v250\n"
    "s_waitcnt lgkmcnt(2)\n"
    "v_accvgpr_read_b32 v244, a160\n"
    "v_accvgpr_read_b32 v245, a161\n"
    "v_accvgpr_read_b32 v246, a162\n"
    "v_accvgpr_read_b32 v247, a163\n"
    "v_lshlrev_b32 v248, 16, v156\n"
    "v_lshlrev_b32 v249, 16, v156\n"
    "v_fmac_f32 v249, v248, v244\n"
    "v_and_b32 v248, 0xffff0000, v156\n"
    "v_and_b32 v250, 0xffff0000, v156\n"
    "v_fmac_f32 v250, v248, v245\n"
    "v_cvt_pk_bf16_f32 v124, v249, v250\n"
    "v_lshlrev_b32 v248, 16, v157\n"
    "v_lshlrev_b32 v249, 16, v157\n"
    "v_fmac_f32 v249, v248, v246\n"
    "v_and_b32 v248, 0xffff0000, v157\n"
    "v_and_b32 v250, 0xffff0000, v157\n"
    "v_fmac_f32 v250, v248, v247\n"
    "v_cvt_pk_bf16_f32 v125, v249, v250\n"
    "s_waitcnt lgkmcnt(1)\n"
    "v_accvgpr_read_b32 v244, a176\n"
    "v_accvgpr_read_b32 v245, a177\n"
    "v_accvgpr_read_b32 v246, a178\n"
    "v_accvgpr_read_b32 v247, a179\n"
    "v_lshlrev_b32 v248, 16, v158\n"
    "v_lshlrev_b32 v249, 16, v158\n"
    "v_fmac_f32 v249, v248, v244\n"
    "v_and_b32 v248, 0xffff0000, v158\n"
    "v_and_b32 v250, 0xffff0000, v158\n"
    "v_fmac_f32 v250, v248, v245\n"
    "v_cvt_pk_bf16_f32 v126, v249, v250\n"
    "v_lshlrev_b32 v248, 16, v159\n"
    "v_lshlrev_b32 v249, 16, v159\n"
    "v_fmac_f32 v249, v248, v246\n"
    "v_and_b32 v248, 0xffff0000, v159\n"
    "v_and_b32 v250, 0xffff0000, v159\n"
    "v_fmac_f32 v250, v248, v247\n"
    "v_cvt_pk_bf16_f32 v127, v249, v250\n"
    "s_waitcnt lgkmcnt(0)\n"
    "v_accvgpr_read_b32 v244, a192\n"
    "v_accvgpr_read_b32 v245, a193\n"
    "v_accvgpr_read_b32 v246, a194\n"
    "v_accvgpr_read_b32 v247, a195\n"
    "v_lshlrev_b32 v248, 16, v160\n"
    "v_lshlrev_b32 v249, 16, v160\n"
    "v_fmac_f32 v249, v248, v244\n"
    "v_and_b32 v248, 0xffff0000, v160\n"
    "v_and_b32 v250, 0xffff0000, v160\n"
    "v_fmac_f32 v250, v248, v245\n"
    "v_cvt_pk_bf16_f32 v128, v249, v250\n"
    "v_lshlrev_b32 v248, 16, v161\n"
    "v_lshlrev_b32 v249, 16, v161\n"
    "v_fmac_f32 v249, v248, v246\n"
    "v_and_b32 v248, 0xffff0000, v161\n"
    "v_and_b32 v250, 0xffff0000, v161\n"
    "v_fmac_f32 v250, v248, v247\n"
    "v_cvt_pk_bf16_f32 v129, v249, v250\n"
    "ds_write_b64 v254, v[104:105] offset:0\n"
    "ds_write_b64 v254, v[106:107] offset:32\n"
    "ds_write_b64 v254, v[108:109] offset:64\n"
    "ds_write_b64 v254, v[110:111] offset:96\n"
    "ds_write_b64 v254, v[112:113] offset:128\n"
    "ds_write_b64 v254, v[114:115] offset:160\n"
    "ds_write_b64 v254, v[116:117] offset:192\n"
    "ds_write_b64 v254, v[118:119] offset:224\n"
    "ds_write_b64 v254, v[120:121] offset:256\n"
    "ds_write_b64 v254, v[122:123] offset:288\n"
    "ds_write_b64 v254, v[124:125] offset:320\n"
    "ds_write_b64 v254, v[126:127] offset:352\n"
    "ds_write_b64 v254, v[128:129] offset:384\n"
    "s_waitcnt lgkmcnt(0)\n"
    "ds_read_b64 v[162:163], v254 offset:26880\n"
    "ds_read_b64 v[164:165], v254 offset:26912\n"
    "ds_read_b64 v[166:167], v254 offset:26944\n"
    "ds_read_b64 v[168:169], v254 offset:26976\n"
    "ds_read_b64 v[170:171], v254 offset:27008\n"
    "ds_read_b64 v[172:173], v254 offset:27040\n"
    "ds_read_b64 v[174:175], v254 offset:27072\n"
    "ds_read_b64 v[176:177], v254 offset:27104\n"
    "ds_read_b64 v[178:179], v254 offset:27136\n"
    "ds_read_b64 v[180:181], v254 offset:27168\n"
    "ds_read_b64 v[182:183], v254 offset:27200\n"
    "ds_read_b64 v[184:185], v254 offset:27232\n"
    "ds_read_b64 v[186:187], v254 offset:27264\n"
    "s_waitcnt lgkmcnt(12)\n"
    "v_accvgpr_read_b32 v244, a4\n"
    "v_accvgpr_read_b32 v245, a5\n"
    "v_accvgpr_read_b32 v246, a6\n"
    "v_accvgpr_read_b32 v247, a7\n"
    "v_lshlrev_b32 v248, 16, v162\n"
    "v_lshlrev_b32 v249, 16, v162\n"
    "v_fmac_f32 v249, v248, v244\n"
    "v_and_b32 v248, 0xffff0000, v162\n"
    "v_and_b32 v250, 0xffff0000, v162\n"
    "v_fmac_f32 v250, v248, v245\n"
    "v_cvt_pk_bf16_f32 v104, v249, v250\n"
    "v_lshlrev_b32 v248, 16, v163\n"
    "v_lshlrev_b32 v249, 16, v163\n"
    "v_fmac_f32 v249, v248, v246\n"
    "v_and_b32 v248, 0xffff0000, v163\n"
    "v_and_b32 v250, 0xffff0000, v163\n"
    "v_fmac_f32 v250, v248, v247\n"
    "v_cvt_pk_bf16_f32 v105, v249, v250\n"
    "s_waitcnt lgkmcnt(11)\n"
    "v_accvgpr_read_b32 v244, a20\n"
    "v_accvgpr_read_b32 v245, a21\n"
    "v_accvgpr_read_b32 v246, a22\n"
    "v_accvgpr_read_b32 v247, a23\n"
    "v_lshlrev_b32 v248, 16, v164\n"
    "v_lshlrev_b32 v249, 16, v164\n"
    "v_fmac_f32 v249, v248, v244\n"
    "v_and_b32 v248, 0xffff0000, v164\n"
    "v_and_b32 v250, 0xffff0000, v164\n"
    "v_fmac_f32 v250, v248, v245\n"
    "v_cvt_pk_bf16_f32 v106, v249, v250\n"
    "v_lshlrev_b32 v248, 16, v165\n"
    "v_lshlrev_b32 v249, 16, v165\n"
    "v_fmac_f32 v249, v248, v246\n"
    "v_and_b32 v248, 0xffff0000, v165\n"
    "v_and_b32 v250, 0xffff0000, v165\n"
    "v_fmac_f32 v250, v248, v247\n"
    "v_cvt_pk_bf16_f32 v107, v249, v250\n"
    "s_waitcnt lgkmcnt(10)\n"
    "v_accvgpr_read_b32 v244, a36\n"
    "v_accvgpr_read_b32 v245, a37\n"
    "v_accvgpr_read_b32 v246, a38\n"
    "v_accvgpr_read_b32 v247, a39\n"
    "v_lshlrev_b32 v248, 16, v166\n"
    "v_lshlrev_b32 v249, 16, v166\n"
    "v_fmac_f32 v249, v248, v244\n"
    "v_and_b32 v248, 0xffff0000, v166\n"
    "v_and_b32 v250, 0xffff0000, v166\n"
    "v_fmac_f32 v250, v248, v245\n"
    "v_cvt_pk_bf16_f32 v108, v249, v250\n"
    "v_lshlrev_b32 v248, 16, v167\n"
    "v_lshlrev_b32 v249, 16, v167\n"
    "v_fmac_f32 v249, v248, v246\n"
    "v_and_b32 v248, 0xffff0000, v167\n"
    "v_and_b32 v250, 0xffff0000, v167\n"
    "v_fmac_f32 v250, v248, v247\n"
    "v_cvt_pk_bf16_f32 v109, v249, v250\n"
    "s_waitcnt lgkmcnt(9)\n"
    "v_accvgpr_read_b32 v244, a52\n"
    "v_accvgpr_read_b32 v245, a53\n"
    "v_accvgpr_read_b32 v246, a54\n"
    "v_accvgpr_read_b32 v247, a55\n"
    "v_lshlrev_b32 v248, 16, v168\n"
    "v_lshlrev_b32 v249, 16, v168\n"
    "v_fmac_f32 v249, v248, v244\n"
    "v_and_b32 v248, 0xffff0000, v168\n"
    "v_and_b32 v250, 0xffff0000, v168\n"
    "v_fmac_f32 v250, v248, v245\n"
    "v_cvt_pk_bf16_f32 v110, v249, v250\n"
    "v_lshlrev_b32 v248, 16, v169\n"
    "v_lshlrev_b32 v249, 16, v169\n"
    "v_fmac_f32 v249, v248, v246\n"
    "v_and_b32 v248, 0xffff0000, v169\n"
    "v_and_b32 v250, 0xffff0000, v169\n"
    "v_fmac_f32 v250, v248, v247\n"
    "v_cvt_pk_bf16_f32 v111, v249, v250\n"
    "s_waitcnt lgkmcnt(8)\n"
    "v_accvgpr_read_b32 v244, a68\n"
    "v_accvgpr_read_b32 v245, a69\n"
    "v_accvgpr_read_b32 v246, a70\n"
    "v_accvgpr_read_b32 v247, a71\n"
    "v_lshlrev_b32 v248, 16, v170\n"
    "v_lshlrev_b32 v249, 16, v170\n"
    "v_fmac_f32 v249, v248, v244\n"
    "v_and_b32 v248, 0xffff0000, v170\n"
    "v_and_b32 v250, 0xffff0000, v170\n"
    "v_fmac_f32 v250, v248, v245\n"
    "v_cvt_pk_bf16_f32 v112, v249, v250\n"
    "v_lshlrev_b32 v248, 16, v171\n"
    "v_lshlrev_b32 v249, 16, v171\n"
    "v_fmac_f32 v249, v248, v246\n"
    "v_and_b32 v248, 0xffff0000, v171\n"
    "v_and_b32 v250, 0xffff0000, v171\n"
    "v_fmac_f32 v250, v248, v247\n"
    "v_cvt_pk_bf16_f32 v113, v249, v250\n"
    "s_waitcnt lgkmcnt(7)\n"
    "v_accvgpr_read_b32 v244, a84\n"
    "v_accvgpr_read_b32 v245, a85\n"
    "v_accvgpr_read_b32 v246, a86\n"
    "v_accvgpr_read_b32 v247, a87\n"
    "v_lshlrev_b32 v248, 16, v172\n"
    "v_lshlrev_b32 v249, 16, v172\n"
    "v_fmac_f32 v249, v248, v244\n"
    "v_and_b32 v248, 0xffff0000, v172\n"
    "v_and_b32 v250, 0xffff0000, v172\n"
    "v_fmac_f32 v250, v248, v245\n"
    "v_cvt_pk_bf16_f32 v114, v249, v250\n"
    "v_lshlrev_b32 v248, 16, v173\n"
    "v_lshlrev_b32 v249, 16, v173\n"
    "v_fmac_f32 v249, v248, v246\n"
    "v_and_b32 v248, 0xffff0000, v173\n"
    "v_and_b32 v250, 0xffff0000, v173\n"
    "v_fmac_f32 v250, v248, v247\n"
    "v_cvt_pk_bf16_f32 v115, v249, v250\n"
    "s_waitcnt lgkmcnt(6)\n"
    "v_accvgpr_read_b32 v244, a100\n"
    "v_accvgpr_read_b32 v245, a101\n"
    "v_accvgpr_read_b32 v246, a102\n"
    "v_accvgpr_read_b32 v247, a103\n"
    "v_lshlrev_b32 v248, 16, v174\n"
    "v_lshlrev_b32 v249, 16, v174\n"
    "v_fmac_f32 v249, v248, v244\n"
    "v_and_b32 v248, 0xffff0000, v174\n"
    "v_and_b32 v250, 0xffff0000, v174\n"
    "v_fmac_f32 v250, v248, v245\n"
    "v_cvt_pk_bf16_f32 v116, v249, v250\n"
    "v_lshlrev_b32 v248, 16, v175\n"
    "v_lshlrev_b32 v249, 16, v175\n"
    "v_fmac_f32 v249, v248, v246\n"
    "v_and_b32 v248, 0xffff0000, v175\n"
    "v_and_b32 v250, 0xffff0000, v175\n"
    "v_fmac_f32 v250, v248, v247\n"
    "v_cvt_pk_bf16_f32 v117, v249, v250\n"
    "s_waitcnt lgkmcnt(5)\n"
    "v_accvgpr_read_b32 v244, a116\n"
    "v_accvgpr_read_b32 v245, a117\n"
    "v_accvgpr_read_b32 v246, a118\n"
    "v_accvgpr_read_b32 v247, a119\n"
    "v_lshlrev_b32 v248, 16, v176\n"
    "v_lshlrev_b32 v249, 16, v176\n"
    "v_fmac_f32 v249, v248, v244\n"
    "v_and_b32 v248, 0xffff0000, v176\n"
    "v_and_b32 v250, 0xffff0000, v176\n"
    "v_fmac_f32 v250, v248, v245\n"
    "v_cvt_pk_bf16_f32 v118, v249, v250\n"
    "v_lshlrev_b32 v248, 16, v177\n"
    "v_lshlrev_b32 v249, 16, v177\n"
    "v_fmac_f32 v249, v248, v246\n"
    "v_and_b32 v248, 0xffff0000, v177\n"
    "v_and_b32 v250, 0xffff0000, v177\n"
    "v_fmac_f32 v250, v248, v247\n"
    "v_cvt_pk_bf16_f32 v119, v249, v250\n"
    "s_waitcnt lgkmcnt(4)\n"
    "v_accvgpr_read_b32 v244, a132\n"
    "v_accvgpr_read_b32 v245, a133\n"
    "v_accvgpr_read_b32 v246, a134\n"
    "v_accvgpr_read_b32 v247, a135\n"
    "v_lshlrev_b32 v248, 16, v178\n"
    "v_lshlrev_b32 v249, 16, v178\n"
    "v_fmac_f32 v249, v248, v244\n"
    "v_and_b32 v248, 0xffff0000, v178\n"
    "v_and_b32 v250, 0xffff0000, v178\n"
    "v_fmac_f32 v250, v248, v245\n"
    "v_cvt_pk_bf16_f32 v120, v249, v250\n"
    "v_lshlrev_b32 v248, 16, v179\n"
    "v_lshlrev_b32 v249, 16, v179\n"
    "v_fmac_f32 v249, v248, v246\n"
    "v_and_b32 v248, 0xffff0000, v179\n"
    "v_and_b32 v250, 0xffff0000, v179\n"
    "v_fmac_f32 v250, v248, v247\n"
    "v_cvt_pk_bf16_f32 v121, v249, v250\n"
    "s_waitcnt lgkmcnt(3)\n"
    "v_accvgpr_read_b32 v244, a148\n"
    "v_accvgpr_read_b32 v245, a149\n"
    "v_accvgpr_read_b32 v246, a150\n"
    "v_accvgpr_read_b32 v247, a151\n"
    "v_lshlrev_b32 v248, 16, v180\n"
    "v_lshlrev_b32 v249, 16, v180\n"
    "v_fmac_f32 v249, v248, v244\n"
    "v_and_b32 v248, 0xffff0000, v180\n"
    "v_and_b32 v250, 0xffff0000, v180\n"
    "v_fmac_f32 v250, v248, v245\n"
    "v_cvt_pk_bf16_f32 v122, v249, v250\n"
    "v_lshlrev_b32 v248, 16, v181\n"
    "v_lshlrev_b32 v249, 16, v181\n"
    "v_fmac_f32 v249, v248, v246\n"
    "v_and_b32 v248, 0xffff0000, v181\n"
    "v_and_b32 v250, 0xffff0000, v181\n"
    "v_fmac_f32 v250, v248, v247\n"
    "v_cvt_pk_bf16_f32 v123, v249, v250\n"
    "s_waitcnt lgkmcnt(2)\n"
    "v_accvgpr_read_b32 v244, a164\n"
    "v_accvgpr_read_b32 v245, a165\n"
    "v_accvgpr_read_b32 v246, a166\n"
    "v_accvgpr_read_b32 v247, a167\n"
    "v_lshlrev_b32 v248, 16, v182\n"
    "v_lshlrev_b32 v249, 16, v182\n"
    "v_fmac_f32 v249, v248, v244\n"
    "v_and_b32 v248, 0xffff0000, v182\n"
    "v_and_b32 v250, 0xffff0000, v182\n"
    "v_fmac_f32 v250, v248, v245\n"
    "v_cvt_pk_bf16_f32 v124, v249, v250\n"
    "v_lshlrev_b32 v248, 16, v183\n"
    "v_lshlrev_b32 v249, 16, v183\n"
    "v_fmac_f32 v249, v248, v246\n"
    "v_and_b32 v248, 0xffff0000, v183\n"
    "v_and_b32 v250, 0xffff0000, v183\n"
    "v_fmac_f32 v250, v248, v247\n"
    "v_cvt_pk_bf16_f32 v125, v249, v250\n"
    "s_waitcnt lgkmcnt(1)\n"
    "v_accvgpr_read_b32 v244, a180\n"
    "v_accvgpr_read_b32 v245, a181\n"
    "v_accvgpr_read_b32 v246, a182\n"
    "v_accvgpr_read_b32 v247, a183\n"
    "v_lshlrev_b32 v248, 16, v184\n"
    "v_lshlrev_b32 v249, 16, v184\n"
    "v_fmac_f32 v249, v248, v244\n"
    "v_and_b32 v248, 0xffff0000, v184\n"
    "v_and_b32 v250, 0xffff0000, v184\n"
    "v_fmac_f32 v250, v248, v245\n"
    "v_cvt_pk_bf16_f32 v126, v249, v250\n"
    "v_lshlrev_b32 v248, 16, v185\n"
    "v_lshlrev_b32 v249, 16, v185\n"
    "v_fmac_f32 v249, v248, v246\n"
    "v_and_b32 v248, 0xffff0000, v185\n"
    "v_and_b32 v250, 0xffff0000, v185\n"
    "v_fmac_f32 v250, v248, v247\n"
    "v_cvt_pk_bf16_f32 v127, v249, v250\n"
    "s_waitcnt lgkmcnt(0)\n"
    "v_accvgpr_read_b32 v244, a196\n"
    "v_accvgpr_read_b32 v245, a197\n"
    "v_accvgpr_read_b32 v246, a198\n"
    "v_accvgpr_read_b32 v247, a199\n"
    "v_lshlrev_b32 v248, 16, v186\n"
    "v_lshlrev_b32 v249, 16, v186\n"
    "v_fmac_f32 v249, v248, v244\n"
    "v_and_b32 v248, 0xffff0000, v186\n"
    "v_and_b32 v250, 0xffff0000, v186\n"
    "v_fmac_f32 v250, v248, v245\n"
    "v_cvt_pk_bf16_f32 v128, v249, v250\n"
    "v_lshlrev_b32 v248, 16, v187\n"
    "v_lshlrev_b32 v249, 16, v187\n"
    "v_fmac_f32 v249, v248, v246\n"
    "v_and_b32 v248, 0xffff0000, v187\n"
    "v_and_b32 v250, 0xffff0000, v187\n"
    "v_fmac_f32 v250, v248, v247\n"
    "v_cvt_pk_bf16_f32 v129, v249, v250\n"
    "ds_write_b64 v254, v[104:105] offset:26880\n"
    "ds_write_b64 v254, v[106:107] offset:26912\n"
    "ds_write_b64 v254, v[108:109] offset:26944\n"
    "ds_write_b64 v254, v[110:111] offset:26976\n"
    "ds_write_b64 v254, v[112:113] offset:27008\n"
    "ds_write_b64 v254, v[114:115] offset:27040\n"
    "ds_write_b64 v254, v[116:117] offset:27072\n"
    "ds_write_b64 v254, v[118:119] offset:27104\n"
    "ds_write_b64 v254, v[120:121] offset:27136\n"
    "ds_write_b64 v254, v[122:123] offset:27168\n"
    "ds_write_b64 v254, v[124:125] offset:27200\n"
    "ds_write_b64 v254, v[126:127] offset:27232\n"
    "ds_write_b64 v254, v[128:129] offset:27264\n"
    "s_waitcnt lgkmcnt(0)\n"
    "ds_read_b64 v[188:189], v255 offset:0\n"
    "ds_read_b64 v[190:191], v255 offset:32\n"
    "ds_read_b64 v[192:193], v255 offset:64\n"
    "ds_read_b64 v[194:195], v255 offset:96\n"
    "ds_read_b64 v[196:197], v255 offset:128\n"
    "ds_read_b64 v[198:199], v255 offset:160\n"
    "ds_read_b64 v[200:201], v255 offset:192\n"
    "ds_read_b64 v[202:203], v255 offset:224\n"
    "ds_read_b64 v[204:205], v255 offset:256\n"
    "ds_read_b64 v[206:207], v255 offset:288\n"
    "ds_read_b64 v[208:209], v255 offset:320\n"
    "ds_read_b64 v[210:211], v255 offset:352\n"
    "ds_read_b64 v[212:213], v255 offset:384\n"
    "s_waitcnt lgkmcnt(12)\n"
    "v_accvgpr_read_b32 v244, a8\n"
    "v_accvgpr_read_b32 v245, a9\n"
    "v_accvgpr_read_b32 v246, a10\n"
    "v_accvgpr_read_b32 v247, a11\n"
    "v_lshlrev_b32 v248, 16, v188\n"
    "v_lshlrev_b32 v249, 16, v188\n"
    "v_fmac_f32 v249, v248, v244\n"
    "v_and_b32 v248, 0xffff0000, v188\n"
    "v_and_b32 v250, 0xffff0000, v188\n"
    "v_fmac_f32 v250, v248, v245\n"
    "v_cvt_pk_bf16_f32 v104, v249, v250\n"
    "v_lshlrev_b32 v248, 16, v189\n"
    "v_lshlrev_b32 v249, 16, v189\n"
    "v_fmac_f32 v249, v248, v246\n"
    "v_and_b32 v248, 0xffff0000, v189\n"
    "v_and_b32 v250, 0xffff0000, v189\n"
    "v_fmac_f32 v250, v248, v247\n"
    "v_cvt_pk_bf16_f32 v105, v249, v250\n"
    "s_waitcnt lgkmcnt(11)\n"
    "v_accvgpr_read_b32 v244, a24\n"
    "v_accvgpr_read_b32 v245, a25\n"
    "v_accvgpr_read_b32 v246, a26\n"
    "v_accvgpr_read_b32 v247, a27\n"
    "v_lshlrev_b32 v248, 16, v190\n"
    "v_lshlrev_b32 v249, 16, v190\n"
    "v_fmac_f32 v249, v248, v244\n"
    "v_and_b32 v248, 0xffff0000, v190\n"
    "v_and_b32 v250, 0xffff0000, v190\n"
    "v_fmac_f32 v250, v248, v245\n"
    "v_cvt_pk_bf16_f32 v106, v249, v250\n"
    "v_lshlrev_b32 v248, 16, v191\n"
    "v_lshlrev_b32 v249, 16, v191\n"
    "v_fmac_f32 v249, v248, v246\n"
    "v_and_b32 v248, 0xffff0000, v191\n"
    "v_and_b32 v250, 0xffff0000, v191\n"
    "v_fmac_f32 v250, v248, v247\n"
    "v_cvt_pk_bf16_f32 v107, v249, v250\n"
    "s_waitcnt lgkmcnt(10)\n"
    "v_accvgpr_read_b32 v244, a40\n"
    "v_accvgpr_read_b32 v245, a41\n"
    "v_accvgpr_read_b32 v246, a42\n"
    "v_accvgpr_read_b32 v247, a43\n"
    "v_lshlrev_b32 v248, 16, v192\n"
    "v_lshlrev_b32 v249, 16, v192\n"
    "v_fmac_f32 v249, v248, v244\n"
    "v_and_b32 v248, 0xffff0000, v192\n"
    "v_and_b32 v250, 0xffff0000, v192\n"
    "v_fmac_f32 v250, v248, v245\n"
    "v_cvt_pk_bf16_f32 v108, v249, v250\n"
    "v_lshlrev_b32 v248, 16, v193\n"
    "v_lshlrev_b32 v249, 16, v193\n"
    "v_fmac_f32 v249, v248, v246\n"
    "v_and_b32 v248, 0xffff0000, v193\n"
    "v_and_b32 v250, 0xffff0000, v193\n"
    "v_fmac_f32 v250, v248, v247\n"
    "v_cvt_pk_bf16_f32 v109, v249, v250\n"
    "s_waitcnt lgkmcnt(9)\n"
    "v_accvgpr_read_b32 v244, a56\n"
    "v_accvgpr_read_b32 v245, a57\n"
    "v_accvgpr_read_b32 v246, a58\n"
    "v_accvgpr_read_b32 v247, a59\n"
    "v_lshlrev_b32 v248, 16, v194\n"
    "v_lshlrev_b32 v249, 16, v194\n"
    "v_fmac_f32 v249, v248, v244\n"
    "v_and_b32 v248, 0xffff0000, v194\n"
    "v_and_b32 v250, 0xffff0000, v194\n"
    "v_fmac_f32 v250, v248, v245\n"
    "v_cvt_pk_bf16_f32 v110, v249, v250\n"
    "v_lshlrev_b32 v248, 16, v195\n"
    "v_lshlrev_b32 v249, 16, v195\n"
    "v_fmac_f32 v249, v248, v246\n"
    "v_and_b32 v248, 0xffff0000, v195\n"
    "v_and_b32 v250, 0xffff0000, v195\n"
    "v_fmac_f32 v250, v248, v247\n"
    "v_cvt_pk_bf16_f32 v111, v249, v250\n"
    "s_waitcnt lgkmcnt(8)\n"
    "v_accvgpr_read_b32 v244, a72\n"
    "v_accvgpr_read_b32 v245, a73\n"
    "v_accvgpr_read_b32 v246, a74\n"
    "v_accvgpr_read_b32 v247, a75\n"
    "v_lshlrev_b32 v248, 16, v196\n"
    "v_lshlrev_b32 v249, 16, v196\n"
    "v_fmac_f32 v249, v248, v244\n"
    "v_and_b32 v248, 0xffff0000, v196\n"
    "v_and_b32 v250, 0xffff0000, v196\n"
    "v_fmac_f32 v250, v248, v245\n"
    "v_cvt_pk_bf16_f32 v112, v249, v250\n"
    "v_lshlrev_b32 v248, 16, v197\n"
    "v_lshlrev_b32 v249, 16, v197\n"
    "v_fmac_f32 v249, v248, v246\n"
    "v_and_b32 v248, 0xffff0000, v197\n"
    "v_and_b32 v250, 0xffff0000, v197\n"
    "v_fmac_f32 v250, v248, v247\n"
    "v_cvt_pk_bf16_f32 v113, v249, v250\n"
    "s_waitcnt lgkmcnt(7)\n"
    "v_accvgpr_read_b32 v244, a88\n"
    "v_accvgpr_read_b32 v245, a89\n"
    "v_accvgpr_read_b32 v246, a90\n"
    "v_accvgpr_read_b32 v247, a91\n"
    "v_lshlrev_b32 v248, 16, v198\n"
    "v_lshlrev_b32 v249, 16, v198\n"
    "v_fmac_f32 v249, v248, v244\n"
    "v_and_b32 v248, 0xffff0000, v198\n"
    "v_and_b32 v250, 0xffff0000, v198\n"
    "v_fmac_f32 v250, v248, v245\n"
    "v_cvt_pk_bf16_f32 v114, v249, v250\n"
    "v_lshlrev_b32 v248, 16, v199\n"
    "v_lshlrev_b32 v249, 16, v199\n"
    "v_fmac_f32 v249, v248, v246\n"
    "v_and_b32 v248, 0xffff0000, v199\n"
    "v_and_b32 v250, 0xffff0000, v199\n"
    "v_fmac_f32 v250, v248, v247\n"
    "v_cvt_pk_bf16_f32 v115, v249, v250\n"
    "s_waitcnt lgkmcnt(6)\n"
    "v_accvgpr_read_b32 v244, a104\n"
    "v_accvgpr_read_b32 v245, a105\n"
    "v_accvgpr_read_b32 v246, a106\n"
    "v_accvgpr_read_b32 v247, a107\n"
    "v_lshlrev_b32 v248, 16, v200\n"
    "v_lshlrev_b32 v249, 16, v200\n"
    "v_fmac_f32 v249, v248, v244\n"
    "v_and_b32 v248, 0xffff0000, v200\n"
    "v_and_b32 v250, 0xffff0000, v200\n"
    "v_fmac_f32 v250, v248, v245\n"
    "v_cvt_pk_bf16_f32 v116, v249, v250\n"
    "v_lshlrev_b32 v248, 16, v201\n"
    "v_lshlrev_b32 v249, 16, v201\n"
    "v_fmac_f32 v249, v248, v246\n"
    "v_and_b32 v248, 0xffff0000, v201\n"
    "v_and_b32 v250, 0xffff0000, v201\n"
    "v_fmac_f32 v250, v248, v247\n"
    "v_cvt_pk_bf16_f32 v117, v249, v250\n"
    "s_waitcnt lgkmcnt(5)\n"
    "v_accvgpr_read_b32 v244, a120\n"
    "v_accvgpr_read_b32 v245, a121\n"
    "v_accvgpr_read_b32 v246, a122\n"
    "v_accvgpr_read_b32 v247, a123\n"
    "v_lshlrev_b32 v248, 16, v202\n"
    "v_lshlrev_b32 v249, 16, v202\n"
    "v_fmac_f32 v249, v248, v244\n"
    "v_and_b32 v248, 0xffff0000, v202\n"
    "v_and_b32 v250, 0xffff0000, v202\n"
    "v_fmac_f32 v250, v248, v245\n"
    "v_cvt_pk_bf16_f32 v118, v249, v250\n"
    "v_lshlrev_b32 v248, 16, v203\n"
    "v_lshlrev_b32 v249, 16, v203\n"
    "v_fmac_f32 v249, v248, v246\n"
    "v_and_b32 v248, 0xffff0000, v203\n"
    "v_and_b32 v250, 0xffff0000, v203\n"
    "v_fmac_f32 v250, v248, v247\n"
    "v_cvt_pk_bf16_f32 v119, v249, v250\n"
    "s_waitcnt lgkmcnt(4)\n"
    "v_accvgpr_read_b32 v244, a136\n"
    "v_accvgpr_read_b32 v245, a137\n"
    "v_accvgpr_read_b32 v246, a138\n"
    "v_accvgpr_read_b32 v247, a139\n"
    "v_lshlrev_b32 v248, 16, v204\n"
    "v_lshlrev_b32 v249, 16, v204\n"
    "v_fmac_f32 v249, v248, v244\n"
    "v_and_b32 v248, 0xffff0000, v204\n"
    "v_and_b32 v250, 0xffff0000, v204\n"
    "v_fmac_f32 v250, v248, v245\n"
    "v_cvt_pk_bf16_f32 v120, v249, v250\n"
    "v_lshlrev_b32 v248, 16, v205\n"
    "v_lshlrev_b32 v249, 16, v205\n"
    "v_fmac_f32 v249, v248, v246\n"
    "v_and_b32 v248, 0xffff0000, v205\n"
    "v_and_b32 v250, 0xffff0000, v205\n"
    "v_fmac_f32 v250, v248, v247\n"
    "v_cvt_pk_bf16_f32 v121, v249, v250\n"
    "s_waitcnt lgkmcnt(3)\n"
    "v_accvgpr_read_b32 v244, a152\n"
    "v_accvgpr_read_b32 v245, a153\n"
    "v_accvgpr_read_b32 v246, a154\n"
    "v_accvgpr_read_b32 v247, a155\n"
    "v_lshlrev_b32 v248, 16, v206\n"
    "v_lshlrev_b32 v249, 16, v206\n"
    "v_fmac_f32 v249, v248, v244\n"
    "v_and_b32 v248, 0xffff0000, v206\n"
    "v_and_b32 v250, 0xffff0000, v206\n"
    "v_fmac_f32 v250, v248, v245\n"
    "v_cvt_pk_bf16_f32 v122, v249, v250\n"
    "v_lshlrev_b32 v248, 16, v207\n"
    "v_lshlrev_b32 v249, 16, v207\n"
    "v_fmac_f32 v249, v248, v246\n"
    "v_and_b32 v248, 0xffff0000, v207\n"
    "v_and_b32 v250, 0xffff0000, v207\n"
    "v_fmac_f32 v250, v248, v247\n"
    "v_cvt_pk_bf16_f32 v123, v249, v250\n"
    "s_waitcnt lgkmcnt(2)\n"
    "v_accvgpr_read_b32 v244, a168\n"
    "v_accvgpr_read_b32 v245, a169\n"
    "v_accvgpr_read_b32 v246, a170\n"
    "v_accvgpr_read_b32 v247, a171\n"
    "v_lshlrev_b32 v248, 16, v208\n"
    "v_lshlrev_b32 v249, 16, v208\n"
    "v_fmac_f32 v249, v248, v244\n"
    "v_and_b32 v248, 0xffff0000, v208\n"
    "v_and_b32 v250, 0xffff0000, v208\n"
    "v_fmac_f32 v250, v248, v245\n"
    "v_cvt_pk_bf16_f32 v124, v249, v250\n"
    "v_lshlrev_b32 v248, 16, v209\n"
    "v_lshlrev_b32 v249, 16, v209\n"
    "v_fmac_f32 v249, v248, v246\n"
    "v_and_b32 v248, 0xffff0000, v209\n"
    "v_and_b32 v250, 0xffff0000, v209\n"
    "v_fmac_f32 v250, v248, v247\n"
    "v_cvt_pk_bf16_f32 v125, v249, v250\n"
    "s_waitcnt lgkmcnt(1)\n"
    "v_accvgpr_read_b32 v244, a184\n"
    "v_accvgpr_read_b32 v245, a185\n"
    "v_accvgpr_read_b32 v246, a186\n"
    "v_accvgpr_read_b32 v247, a187\n"
    "v_lshlrev_b32 v248, 16, v210\n"
    "v_lshlrev_b32 v249, 16, v210\n"
    "v_fmac_f32 v249, v248, v244\n"
    "v_and_b32 v248, 0xffff0000, v210\n"
    "v_and_b32 v250, 0xffff0000, v210\n"
    "v_fmac_f32 v250, v248, v245\n"
    "v_cvt_pk_bf16_f32 v126, v249, v250\n"
    "v_lshlrev_b32 v248, 16, v211\n"
    "v_lshlrev_b32 v249, 16, v211\n"
    "v_fmac_f32 v249, v248, v246\n"
    "v_and_b32 v248, 0xffff0000, v211\n"
    "v_and_b32 v250, 0xffff0000, v211\n"
    "v_fmac_f32 v250, v248, v247\n"
    "v_cvt_pk_bf16_f32 v127, v249, v250\n"
    "s_waitcnt lgkmcnt(0)\n"
    "v_accvgpr_read_b32 v244, a200\n"
    "v_accvgpr_read_b32 v245, a201\n"
    "v_accvgpr_read_b32 v246, a202\n"
    "v_accvgpr_read_b32 v247, a203\n"
    "v_lshlrev_b32 v248, 16, v212\n"
    "v_lshlrev_b32 v249, 16, v212\n"
    "v_fmac_f32 v249, v248, v244\n"
    "v_and_b32 v248, 0xffff0000, v212\n"
    "v_and_b32 v250, 0xffff0000, v212\n"
    "v_fmac_f32 v250, v248, v245\n"
    "v_cvt_pk_bf16_f32 v128, v249, v250\n"
    "v_lshlrev_b32 v248, 16, v213\n"
    "v_lshlrev_b32 v249, 16, v213\n"
    "v_fmac_f32 v249, v248, v246\n"
    "v_and_b32 v248, 0xffff0000, v213\n"
    "v_and_b32 v250, 0xffff0000, v213\n"
    "v_fmac_f32 v250, v248, v247\n"
    "v_cvt_pk_bf16_f32 v129, v249, v250\n"
    "ds_write_b64 v255, v[104:105] offset:0\n"
    "ds_write_b64 v255, v[106:107] offset:32\n"
    "ds_write_b64 v255, v[108:109] offset:64\n"
    "ds_write_b64 v255, v[110:111] offset:96\n"
    "ds_write_b64 v255, v[112:113] offset:128\n"
    "ds_write_b64 v255, v[114:115] offset:160\n"
    "ds_write_b64 v255, v[116:117] offset:192\n"
    "ds_write_b64 v255, v[118:119] offset:224\n"
    "ds_write_b64 v255, v[120:121] offset:256\n"
    "ds_write_b64 v255, v[122:123] offset:288\n"
    "ds_write_b64 v255, v[124:125] offset:320\n"
    "ds_write_b64 v255, v[126:127] offset:352\n"
    "ds_write_b64 v255, v[128:129] offset:384\n"
    "s_waitcnt lgkmcnt(0)\n"
    "ds_read_b64 v[214:215], v255 offset:26880\n"
    "ds_read_b64 v[216:217], v255 offset:26912\n"
    "ds_read_b64 v[218:219], v255 offset:26944\n"
    "ds_read_b64 v[220:221], v255 offset:26976\n"
    "ds_read_b64 v[222:223], v255 offset:27008\n"
    "ds_read_b64 v[224:225], v255 offset:27040\n"
    "ds_read_b64 v[226:227], v255 offset:27072\n"
    "ds_read_b64 v[228:229], v255 offset:27104\n"
    "ds_read_b64 v[230:231], v255 offset:27136\n"
    "ds_read_b64 v[232:233], v255 offset:27168\n"
    "ds_read_b64 v[234:235], v255 offset:27200\n"
    "ds_read_b64 v[236:237], v255 offset:27232\n"
    "ds_read_b64 v[238:239], v255 offset:27264\n"
    "s_waitcnt lgkmcnt(12)\n"
    "v_accvgpr_read_b32 v244, a12\n"
    "v_accvgpr_read_b32 v245, a13\n"
    "v_accvgpr_read_b32 v246, a14\n"
    "v_accvgpr_read_b32 v247, a15\n"
    "v_lshlrev_b32 v248, 16, v214\n"
    "v_lshlrev_b32 v249, 16, v214\n"
    "v_fmac_f32 v249, v248, v244\n"
    "v_and_b32 v248, 0xffff0000, v214\n"
    "v_and_b32 v250, 0xffff0000, v214\n"
    "v_fmac_f32 v250, v248, v245\n"
    "v_cvt_pk_bf16_f32 v104, v249, v250\n"
    "v_lshlrev_b32 v248, 16, v215\n"
    "v_lshlrev_b32 v249, 16, v215\n"
    "v_fmac_f32 v249, v248, v246\n"
    "v_and_b32 v248, 0xffff0000, v215\n"
    "v_and_b32 v250, 0xffff0000, v215\n"
    "v_fmac_f32 v250, v248, v247\n"
    "v_cvt_pk_bf16_f32 v105, v249, v250\n"
    "s_waitcnt lgkmcnt(11)\n"
    "v_accvgpr_read_b32 v244, a28\n"
    "v_accvgpr_read_b32 v245, a29\n"
    "v_accvgpr_read_b32 v246, a30\n"
    "v_accvgpr_read_b32 v247, a31\n"
    "v_lshlrev_b32 v248, 16, v216\n"
    "v_lshlrev_b32 v249, 16, v216\n"
    "v_fmac_f32 v249, v248, v244\n"
    "v_and_b32 v248, 0xffff0000, v216\n"
    "v_and_b32 v250, 0xffff0000, v216\n"
    "v_fmac_f32 v250, v248, v245\n"
    "v_cvt_pk_bf16_f32 v106, v249, v250\n"
    "v_lshlrev_b32 v248, 16, v217\n"
    "v_lshlrev_b32 v249, 16, v217\n"
    "v_fmac_f32 v249, v248, v246\n"
    "v_and_b32 v248, 0xffff0000, v217\n"
    "v_and_b32 v250, 0xffff0000, v217\n"
    "v_fmac_f32 v250, v248, v247\n"
    "v_cvt_pk_bf16_f32 v107, v249, v250\n"
    "s_waitcnt lgkmcnt(10)\n"
    "v_accvgpr_read_b32 v244, a44\n"
    "v_accvgpr_read_b32 v245, a45\n"
    "v_accvgpr_read_b32 v246, a46\n"
    "v_accvgpr_read_b32 v247, a47\n"
    "v_lshlrev_b32 v248, 16, v218\n"
    "v_lshlrev_b32 v249, 16, v218\n"
    "v_fmac_f32 v249, v248, v244\n"
    "v_and_b32 v248, 0xffff0000, v218\n"
    "v_and_b32 v250, 0xffff0000, v218\n"
    "v_fmac_f32 v250, v248, v245\n"
    "v_cvt_pk_bf16_f32 v108, v249, v250\n"
    "v_lshlrev_b32 v248, 16, v219\n"
    "v_lshlrev_b32 v249, 16, v219\n"
    "v_fmac_f32 v249, v248, v246\n"
    "v_and_b32 v248, 0xffff0000, v219\n"
    "v_and_b32 v250, 0xffff0000, v219\n"
    "v_fmac_f32 v250, v248, v247\n"
    "v_cvt_pk_bf16_f32 v109, v249, v250\n"
    "s_waitcnt lgkmcnt(9)\n"
    "v_accvgpr_read_b32 v244, a60\n"
    "v_accvgpr_read_b32 v245, a61\n"
    "v_accvgpr_read_b32 v246, a62\n"
    "v_accvgpr_read_b32 v247, a63\n"
    "v_lshlrev_b32 v248, 16, v220\n"
    "v_lshlrev_b32 v249, 16, v220\n"
    "v_fmac_f32 v249, v248, v244\n"
    "v_and_b32 v248, 0xffff0000, v220\n"
    "v_and_b32 v250, 0xffff0000, v220\n"
    "v_fmac_f32 v250, v248, v245\n"
    "v_cvt_pk_bf16_f32 v110, v249, v250\n"
    "v_lshlrev_b32 v248, 16, v221\n"
    "v_lshlrev_b32 v249, 16, v221\n"
    "v_fmac_f32 v249, v248, v246\n"
    "v_and_b32 v248, 0xffff0000, v221\n"
    "v_and_b32 v250, 0xffff0000, v221\n"
    "v_fmac_f32 v250, v248, v247\n"
    "v_cvt_pk_bf16_f32 v111, v249, v250\n"
    "s_waitcnt lgkmcnt(8)\n"
    "v_accvgpr_read_b32 v244, a76\n"
    "v_accvgpr_read_b32 v245, a77\n"
    "v_accvgpr_read_b32 v246, a78\n"
    "v_accvgpr_read_b32 v247, a79\n"
    "v_lshlrev_b32 v248, 16, v222\n"
    "v_lshlrev_b32 v249, 16, v222\n"
    "v_fmac_f32 v249, v248, v244\n"
    "v_and_b32 v248, 0xffff0000, v222\n"
    "v_and_b32 v250, 0xffff0000, v222\n"
    "v_fmac_f32 v250, v248, v245\n"
    "v_cvt_pk_bf16_f32 v112, v249, v250\n"
    "v_lshlrev_b32 v248, 16, v223\n"
    "v_lshlrev_b32 v249, 16, v223\n"
    "v_fmac_f32 v249, v248, v246\n"
    "v_and_b32 v248, 0xffff0000, v223\n"
    "v_and_b32 v250, 0xffff0000, v223\n"
    "v_fmac_f32 v250, v248, v247\n"
    "v_cvt_pk_bf16_f32 v113, v249, v250\n"
    "s_waitcnt lgkmcnt(7)\n"
    "v_accvgpr_read_b32 v244, a92\n"
    "v_accvgpr_read_b32 v245, a93\n"
    "v_accvgpr_read_b32 v246, a94\n"
    "v_accvgpr_read_b32 v247, a95\n"
    "v_lshlrev_b32 v248, 16, v224\n"
    "v_lshlrev_b32 v249, 16, v224\n"
    "v_fmac_f32 v249, v248, v244\n"
    "v_and_b32 v248, 0xffff0000, v224\n"
    "v_and_b32 v250, 0xffff0000, v224\n"
    "v_fmac_f32 v250, v248, v245\n"
    "v_cvt_pk_bf16_f32 v114, v249, v250\n"
    "v_lshlrev_b32 v248, 16, v225\n"
    "v_lshlrev_b32 v249, 16, v225\n"
    "v_fmac_f32 v249, v248, v246\n"
    "v_and_b32 v248, 0xffff0000, v225\n"
    "v_and_b32 v250, 0xffff0000, v225\n"
    "v_fmac_f32 v250, v248, v247\n"
    "v_cvt_pk_bf16_f32 v115, v249, v250\n"
    "s_waitcnt lgkmcnt(6)\n"
    "v_accvgpr_read_b32 v244, a108\n"
    "v_accvgpr_read_b32 v245, a109\n"
    "v_accvgpr_read_b32 v246, a110\n"
    "v_accvgpr_read_b32 v247, a111\n"
    "v_lshlrev_b32 v248, 16, v226\n"
    "v_lshlrev_b32 v249, 16, v226\n"
    "v_fmac_f32 v249, v248, v244\n"
    "v_and_b32 v248, 0xffff0000, v226\n"
    "v_and_b32 v250, 0xffff0000, v226\n"
    "v_fmac_f32 v250, v248, v245\n"
    "v_cvt_pk_bf16_f32 v116, v249, v250\n"
    "v_lshlrev_b32 v248, 16, v227\n"
    "v_lshlrev_b32 v249, 16, v227\n"
    "v_fmac_f32 v249, v248, v246\n"
    "v_and_b32 v248, 0xffff0000, v227\n"
    "v_and_b32 v250, 0xffff0000, v227\n"
    "v_fmac_f32 v250, v248, v247\n"
    "v_cvt_pk_bf16_f32 v117, v249, v250\n"
    "s_waitcnt lgkmcnt(5)\n"
    "v_accvgpr_read_b32 v244, a124\n"
    "v_accvgpr_read_b32 v245, a125\n"
    "v_accvgpr_read_b32 v246, a126\n"
    "v_accvgpr_read_b32 v247, a127\n"
    "v_lshlrev_b32 v248, 16, v228\n"
    "v_lshlrev_b32 v249, 16, v228\n"
    "v_fmac_f32 v249, v248, v244\n"
    "v_and_b32 v248, 0xffff0000, v228\n"
    "v_and_b32 v250, 0xffff0000, v228\n"
    "v_fmac_f32 v250, v248, v245\n"
    "v_cvt_pk_bf16_f32 v118, v249, v250\n"
    "v_lshlrev_b32 v248, 16, v229\n"
    "v_lshlrev_b32 v249, 16, v229\n"
    "v_fmac_f32 v249, v248, v246\n"
    "v_and_b32 v248, 0xffff0000, v229\n"
    "v_and_b32 v250, 0xffff0000, v229\n"
    "v_fmac_f32 v250, v248, v247\n"
    "v_cvt_pk_bf16_f32 v119, v249, v250\n"
    "s_waitcnt lgkmcnt(4)\n"
    "v_accvgpr_read_b32 v244, a140\n"
    "v_accvgpr_read_b32 v245, a141\n"
    "v_accvgpr_read_b32 v246, a142\n"
    "v_accvgpr_read_b32 v247, a143\n"
    "v_lshlrev_b32 v248, 16, v230\n"
    "v_lshlrev_b32 v249, 16, v230\n"
    "v_fmac_f32 v249, v248, v244\n"
    "v_and_b32 v248, 0xffff0000, v230\n"
    "v_and_b32 v250, 0xffff0000, v230\n"
    "v_fmac_f32 v250, v248, v245\n"
    "v_cvt_pk_bf16_f32 v120, v249, v250\n"
    "v_lshlrev_b32 v248, 16, v231\n"
    "v_lshlrev_b32 v249, 16, v231\n"
    "v_fmac_f32 v249, v248, v246\n"
    "v_and_b32 v248, 0xffff0000, v231\n"
    "v_and_b32 v250, 0xffff0000, v231\n"
    "v_fmac_f32 v250, v248, v247\n"
    "v_cvt_pk_bf16_f32 v121, v249, v250\n"
    "s_waitcnt lgkmcnt(3)\n"
    "v_accvgpr_read_b32 v244, a156\n"
    "v_accvgpr_read_b32 v245, a157\n"
    "v_accvgpr_read_b32 v246, a158\n"
    "v_accvgpr_read_b32 v247, a159\n"
    "v_lshlrev_b32 v248, 16, v232\n"
    "v_lshlrev_b32 v249, 16, v232\n"
    "v_fmac_f32 v249, v248, v244\n"
    "v_and_b32 v248, 0xffff0000, v232\n"
    "v_and_b32 v250, 0xffff0000, v232\n"
    "v_fmac_f32 v250, v248, v245\n"
    "v_cvt_pk_bf16_f32 v122, v249, v250\n"
    "v_lshlrev_b32 v248, 16, v233\n"
    "v_lshlrev_b32 v249, 16, v233\n"
    "v_fmac_f32 v249, v248, v246\n"
    "v_and_b32 v248, 0xffff0000, v233\n"
    "v_and_b32 v250, 0xffff0000, v233\n"
    "v_fmac_f32 v250, v248, v247\n"
    "v_cvt_pk_bf16_f32 v123, v249, v250\n"
    "s_waitcnt lgkmcnt(2)\n"
    "v_accvgpr_read_b32 v244, a172\n"
    "v_accvgpr_read_b32 v245, a173\n"
    "v_accvgpr_read_b32 v246, a174\n"
    "v_accvgpr_read_b32 v247, a175\n"
    "v_lshlrev_b32 v248, 16, v234\n"
    "v_lshlrev_b32 v249, 16, v234\n"
    "v_fmac_f32 v249, v248, v244\n"
    "v_and_b32 v248, 0xffff0000, v234\n"
    "v_and_b32 v250, 0xffff0000, v234\n"
    "v_fmac_f32 v250, v248, v245\n"
    "v_cvt_pk_bf16_f32 v124, v249, v250\n"
    "v_lshlrev_b32 v248, 16, v235\n"
    "v_lshlrev_b32 v249, 16, v235\n"
    "v_fmac_f32 v249, v248, v246\n"
    "v_and_b32 v248, 0xffff0000, v235\n"
    "v_and_b32 v250, 0xffff0000, v235\n"
    "v_fmac_f32 v250, v248, v247\n"
    "v_cvt_pk_bf16_f32 v125, v249, v250\n"
    "s_waitcnt lgkmcnt(1)\n"
    "v_accvgpr_read_b32 v244, a188\n"
    "v_accvgpr_read_b32 v245, a189\n"
    "v_accvgpr_read_b32 v246, a190\n"
    "v_accvgpr_read_b32 v247, a191\n"
    "v_lshlrev_b32 v248, 16, v236\n"
    "v_lshlrev_b32 v249, 16, v236\n"
    "v_fmac_f32 v249, v248, v244\n"
    "v_and_b32 v248, 0xffff0000, v236\n"
    "v_and_b32 v250, 0xffff0000, v236\n"
    "v_fmac_f32 v250, v248, v245\n"
    "v_cvt_pk_bf16_f32 v126, v249, v250\n"
    "v_lshlrev_b32 v248, 16, v237\n"
    "v_lshlrev_b32 v249, 16, v237\n"
    "v_fmac_f32 v249, v248, v246\n"
    "v_and_b32 v248, 0xffff0000, v237\n"
    "v_and_b32 v250, 0xffff0000, v237\n"
    "v_fmac_f32 v250, v248, v247\n"
    "v_cvt_pk_bf16_f32 v127, v249, v250\n"
    "s_waitcnt lgkmcnt(0)\n"
    "v_accvgpr_read_b32 v244, a204\n"
    "v_accvgpr_read_b32 v245, a205\n"
    "v_accvgpr_read_b32 v246, a206\n"
    "v_accvgpr_read_b32 v247, a207\n"
    "v_lshlrev_b32 v248, 16, v238\n"
    "v_lshlrev_b32 v249, 16, v238\n"
    "v_fmac_f32 v249, v248, v244\n"
    "v_and_b32 v248, 0xffff0000, v238\n"
    "v_and_b32 v250, 0xffff0000, v238\n"
    "v_fmac_f32 v250, v248, v245\n"
    "v_cvt_pk_bf16_f32 v128, v249, v250\n"
    "v_lshlrev_b32 v248, 16, v239\n"
    "v_lshlrev_b32 v249, 16, v239\n"
    "v_fmac_f32 v249, v248, v246\n"
    "v_and_b32 v248, 0xffff0000, v239\n"
    "v_and_b32 v250, 0xffff0000, v239\n"
    "v_fmac_f32 v250, v248, v247\n"
    "v_cvt_pk_bf16_f32 v129, v249, v250\n"
    "ds_write_b64 v255, v[104:105] offset:26880\n"
    "ds_write_b64 v255, v[106:107] offset:26912\n"
    "ds_write_b64 v255, v[108:109] offset:26944\n"
    "ds_write_b64 v255, v[110:111] offset:26976\n"
    "ds_write_b64 v255, v[112:113] offset:27008\n"
    "ds_write_b64 v255, v[114:115] offset:27040\n"
    "ds_write_b64 v255, v[116:117] offset:27072\n"
    "ds_write_b64 v255, v[118:119] offset:27104\n"
    "ds_write_b64 v255, v[120:121] offset:27136\n"
    "ds_write_b64 v255, v[122:123] offset:27168\n"
    "ds_write_b64 v255, v[124:125] offset:27200\n"
    "ds_write_b64 v255, v[126:127] offset:27232\n"
    "ds_write_b64 v255, v[128:129] offset:27264\n"
    "s_waitcnt lgkmcnt(0)\n"
    "v_add_u32 v253, 1664, v253\n"
    "global_load_dwordx2 v[104:105], v253, s[52:53] offset:0\n"
    "global_load_dwordx2 v[106:107], v253, s[52:53] offset:32\n"
    "global_load_dwordx2 v[108:109], v253, s[52:53] offset:64\n"
    "global_load_dwordx2 v[110:111], v253, s[52:53] offset:96\n"
    "global_load_dwordx2 v[112:113], v253, s[52:53] offset:128\n"
    "global_load_dwordx2 v[114:115], v253, s[52:53] offset:160\n"
    "global_load_dwordx2 v[116:117], v253, s[52:53] offset:192\n"
    "global_load_dwordx2 v[118:119], v253, s[52:53] offset:224\n"
    "global_load_dwordx2 v[120:121], v253, s[52:53] offset:256\n"
    "global_load_dwordx2 v[122:123], v253, s[52:53] offset:288\n"
    "global_load_dwordx2 v[124:125], v253, s[52:53] offset:320\n"
    "global_load_dwordx2 v[126:127], v253, s[52:53] offset:352\n"
    "global_load_dwordx2 v[128:129], v253, s[52:53] offset:384\n"
    "s_barrier\n"
    "; ===================== layer 1 =====================\n"
    "s_waitcnt vmcnt(0)\n"
    "v_lshlrev_b32 v244, 16, v104\n"
    "v_and_b32 v245, 0xffff0000, v104\n"
    "v_lshlrev_b32 v246, 16, v105\n"
    "v_and_b32 v247, 0xffff0000, v105\n"
    "v_accvgpr_write_b32 a0, v244\n"
    "v_accvgpr_write_b32 a1, v245\n"
    "v_accvgpr_write_b32 a2, v246\n"
    "v_accvgpr_write_b32 a3, v247\n"
    "v_accvgpr_write_b32 a4, v244\n"
    "v_accvgpr_write_b32 a5, v245\n"
    "v_accvgpr_write_b32 a6, v246\n"
    "v_accvgpr_write_b32 a7, v247\n"
    "v_accvgpr_write_b32 a8, v244\n"
    "v_accvgpr_write_b32 a9, v245\n"
    "v_accvgpr_write_b32 a10, v246\n"
    "v_accvgpr_write_b32 a11, v247\n"
    "v_accvgpr_write_b32 a12, v244\n"
    "v_accvgpr_write_b32 a13, v245\n"
    "v_accvgpr_write_b32 a14, v246\n"
    "v_accvgpr_write_b32 a15, v247\n"
    "v_lshlrev_b32 v244, 16, v106\n"
    "v_and_b32 v245, 0xffff0000, v106\n"
    "v_lshlrev_b32 v246, 16, v107\n"
    "v_and_b32 v247, 0xffff0000, v107\n"
    "v_accvgpr_write_b32 a16, v244\n"
    "v_accvgpr_write_b32 a17, v245\n"
    "v_accvgpr_write_b32 a18, v246\n"
    "v_accvgpr_write_b32 a19, v247\n"
    "v_accvgpr_write_b32 a20, v244\n"
    "v_accvgpr_write_b32 a21, v245\n"
    "v_accvgpr_write_b32 a22, v246\n"
    "v_accvgpr_write_b32 a23, v247\n"
    "v_accvgpr_write_b32 a24, v244\n"
    "v_accvgpr_write_b32 a25, v245\n"
    "v_accvgpr_write_b32 a26, v246\n"
    "v_accvgpr_write_b32 a27, v247\n"
    "v_accvgpr_write_b32 a28, v244\n"
    "v_accvgpr_write_b32 a29, v245\n"
    "v_accvgpr_write_b32 a30, v246\n"
    "v_accvgpr_write_b32 a31, v247\n"
    "v_lshlrev_b32 v244, 16, v108\n"
    "v_and_b32 v245, 0xffff0000, v108\n"
    "v_lshlrev_b32 v246, 16, v109\n"
    "v_and_b32 v247, 0xffff0000, v109\n"
    "v_accvgpr_write_b32 a32, v244\n"
    "v_accvgpr_write_b32 a33, v245\n"
    "v_accvgpr_write_b32 a34, v246\n"
    "v_accvgpr_write_b32 a35, v247\n"
    "v_accvgpr_write_b32 a36, v244\n"
    "v_accvgpr_write_b32 a37, v245\n"
    "v_accvgpr_write_b32 a38, v246\n"
    "v_accvgpr_write_b32 a39, v247\n"
    "v_accvgpr_write_b32 a40, v244\n"
    "v_accvgpr_write_b32 a41, v245\n"
    "v_accvgpr_write_b32 a42, v246\n"
    "v_accvgpr_write_b32 a43, v247\n"
    "v_accvgpr_write_b32 a44, v244\n"
    "v_accvgpr_write_b32 a45, v245\n"
    "v_accvgpr_write_b32 a46, v246\n"
    "v_accvgpr_write_b32 a47, v247\n"
    "v_lshlrev_b32 v244, 16, v110\n"
    "v_and_b32 v245, 0xffff0000, v110\n"
    "v_lshlrev_b32 v246, 16, v111\n"
    "v_and_b32 v247, 0xffff0000, v111\n"
    "v_accvgpr_write_b32 a48, v244\n"
    "v_accvgpr_write_b32 a49, v245\n"
    "v_accvgpr_write_b32 a50, v246\n"
    "v_accvgpr_write_b32 a51, v247\n"
    "v_accvgpr_write_b32 a52, v244\n"
    "v_accvgpr_write_b32 a53, v245\n"
    "v_accvgpr_write_b32 a54, v246\n"
    "v_accvgpr_write_b32 a55, v247\n"
    "v_accvgpr_write_b32 a56, v244\n"
    "v_accvgpr_write_b32 a57, v245\n"
    "v_accvgpr_write_b32 a58, v246\n"
    "v_accvgpr_write_b32 a59, v247\n"
    "v_accvgpr_write_b32 a60, v244\n"
    "v_accvgpr_write_b32 a61, v245\n"
    "v_accvgpr_write_b32 a62, v246\n"
    "v_accvgpr_write_b32 a63, v247\n"
    "v_lshlrev_b32 v244, 16, v112\n"
    "v_and_b32 v245, 0xffff0000, v112\n"
    "v_lshlrev_b32 v246, 16, v113\n"
    "v_and_b32 v247, 0xffff0000, v113\n"
    "v_accvgpr_write_b32 a64, v244\n"
    "v_accvgpr_write_b32 a65, v245\n"
    "v_accvgpr_write_b32 a66, v246\n"
    "v_accvgpr_write_b32 a67, v247\n"
    "v_accvgpr_write_b32 a68, v244\n"
    "v_accvgpr_write_b32 a69, v245\n"
    "v_accvgpr_write_b32 a70, v246\n"
    "v_accvgpr_write_b32 a71, v247\n"
    "v_accvgpr_write_b32 a72, v244\n"
    "v_accvgpr_write_b32 a73, v245\n"
    "v_accvgpr_write_b32 a74, v246\n"
    "v_accvgpr_write_b32 a75, v247\n"
    "v_accvgpr_write_b32 a76, v244\n"
    "v_accvgpr_write_b32 a77, v245\n"
    "v_accvgpr_write_b32 a78, v246\n"
    "v_accvgpr_write_b32 a79, v247\n"
    "v_lshlrev_b32 v244, 16, v114\n"
    "v_and_b32 v245, 0xffff0000, v114\n"
    "v_lshlrev_b32 v246, 16, v115\n"
    "v_and_b32 v247, 0xffff0000, v115\n"
    "v_accvgpr_write_b32 a80, v244\n"
    "v_accvgpr_write_b32 a81, v245\n"
    "v_accvgpr_write_b32 a82, v246\n"
    "v_accvgpr_write_b32 a83, v247\n"
    "v_accvgpr_write_b32 a84, v244\n"
    "v_accvgpr_write_b32 a85, v245\n"
    "v_accvgpr_write_b32 a86, v246\n"
    "v_accvgpr_write_b32 a87, v247\n"
    "v_accvgpr_write_b32 a88, v244\n"
    "v_accvgpr_write_b32 a89, v245\n"
    "v_accvgpr_write_b32 a90, v246\n"
    "v_accvgpr_write_b32 a91, v247\n"
    "v_accvgpr_write_b32 a92, v244\n"
    "v_accvgpr_write_b32 a93, v245\n"
    "v_accvgpr_write_b32 a94, v246\n"
    "v_accvgpr_write_b32 a95, v247\n"
    "v_lshlrev_b32 v244, 16, v116\n"
    "v_and_b32 v245, 0xffff0000, v116\n"
    "v_lshlrev_b32 v246, 16, v117\n"
    "v_and_b32 v247, 0xffff0000, v117\n"
    "v_accvgpr_write_b32 a96, v244\n"
    "v_accvgpr_write_b32 a97, v245\n"
    "v_accvgpr_write_b32 a98, v246\n"
    "v_accvgpr_write_b32 a99, v247\n"
    "v_accvgpr_write_b32 a100, v244\n"
    "v_accvgpr_write_b32 a101, v245\n"
    "v_accvgpr_write_b32 a102, v246\n"
    "v_accvgpr_write_b32 a103, v247\n"
    "v_accvgpr_write_b32 a104, v244\n"
    "v_accvgpr_write_b32 a105, v245\n"
    "v_accvgpr_write_b32 a106, v246\n"
    "v_accvgpr_write_b32 a107, v247\n"
    "v_accvgpr_write_b32 a108, v244\n"
    "v_accvgpr_write_b32 a109, v245\n"
    "v_accvgpr_write_b32 a110, v246\n"
    "v_accvgpr_write_b32 a111, v247\n"
    "v_lshlrev_b32 v244, 16, v118\n"
    "v_and_b32 v245, 0xffff0000, v118\n"
    "v_lshlrev_b32 v246, 16, v119\n"
    "v_and_b32 v247, 0xffff0000, v119\n"
    "v_accvgpr_write_b32 a112, v244\n"
    "v_accvgpr_write_b32 a113, v245\n"
    "v_accvgpr_write_b32 a114, v246\n"
    "v_accvgpr_write_b32 a115, v247\n"
    "v_accvgpr_write_b32 a116, v244\n"
    "v_accvgpr_write_b32 a117, v245\n"
    "v_accvgpr_write_b32 a118, v246\n"
    "v_accvgpr_write_b32 a119, v247\n"
    "v_accvgpr_write_b32 a120, v244\n"
    "v_accvgpr_write_b32 a121, v245\n"
    "v_accvgpr_write_b32 a122, v246\n"
    "v_accvgpr_write_b32 a123, v247\n"
    "v_accvgpr_write_b32 a124, v244\n"
    "v_accvgpr_write_b32 a125, v245\n"
    "v_accvgpr_write_b32 a126, v246\n"
    "v_accvgpr_write_b32 a127, v247\n"
    "v_lshlrev_b32 v244, 16, v120\n"
    "v_and_b32 v245, 0xffff0000, v120\n"
    "v_lshlrev_b32 v246, 16, v121\n"
    "v_and_b32 v247, 0xffff0000, v121\n"
    "v_accvgpr_write_b32 a128, v244\n"
    "v_accvgpr_write_b32 a129, v245\n"
    "v_accvgpr_write_b32 a130, v246\n"
    "v_accvgpr_write_b32 a131, v247\n"
    "v_accvgpr_write_b32 a132, v244\n"
    "v_accvgpr_write_b32 a133, v245\n"
    "v_accvgpr_write_b32 a134, v246\n"
    "v_accvgpr_write_b32 a135, v247\n"
    "v_accvgpr_write_b32 a136, v244\n"
    "v_accvgpr_write_b32 a137, v245\n"
    "v_accvgpr_write_b32 a138, v246\n"
    "v_accvgpr_write_b32 a139, v247\n"
    "v_accvgpr_write_b32 a140, v244\n"
    "v_accvgpr_write_b32 a141, v245\n"
    "v_accvgpr_write_b32 a142, v246\n"
    "v_accvgpr_write_b32 a143, v247\n"
    "v_lshlrev_b32 v244, 16, v122\n"
    "v_and_b32 v245, 0xffff0000, v122\n"
    "v_lshlrev_b32 v246, 16, v123\n"
    "v_and_b32 v247, 0xffff0000, v123\n"
    "v_accvgpr_write_b32 a144, v244\n"
    "v_accvgpr_write_b32 a145, v245\n"
    "v_accvgpr_write_b32 a146, v246\n"
    "v_accvgpr_write_b32 a147, v247\n"
    "v_accvgpr_write_b32 a148, v244\n"
    "v_accvgpr_write_b32 a149, v245\n"
    "v_accvgpr_write_b32 a150, v246\n"
    "v_accvgpr_write_b32 a151, v247\n"
    "v_accvgpr_write_b32 a152, v244\n"
    "v_accvgpr_write_b32 a153, v245\n"
    "v_accvgpr_write_b32 a154, v246\n"
    "v_accvgpr_write_b32 a155, v247\n"
    "v_accvgpr_write_b32 a156, v244\n"
    "v_accvgpr_write_b32 a157, v245\n"
    "v_accvgpr_write_b32 a158, v246\n"
    "v_accvgpr_write_b32 a159, v247\n"
    "v_lshlrev_b32 v244, 16, v124\n"
    "v_and_b32 v245, 0xffff0000, v124\n"
    "v_lshlrev_b32 v246, 16, v125\n"
    "v_and_b32 v247, 0xffff0000, v125\n"
    "v_accvgpr_write_b32 a160, v244\n"
    "v_accvgpr_write_b32 a161, v245\n"
    "v_accvgpr_write_b32 a162, v246\n"
    "v_accvgpr_write_b32 a163, v247\n"
    "v_accvgpr_write_b32 a164, v244\n"
    "v_accvgpr_write_b32 a165, v245\n"
    "v_accvgpr_write_b32 a166, v246\n"
    "v_accvgpr_write_b32 a167, v247\n"
    "v_accvgpr_write_b32 a168, v244\n"
    "v_accvgpr_write_b32 a169, v245\n"
    "v_accvgpr_write_b32 a170, v246\n"
    "v_accvgpr_write_b32 a171, v247\n"
    "v_accvgpr_write_b32 a172, v244\n"
    "v_accvgpr_write_b32 a173, v245\n"
    "v_accvgpr_write_b32 a174, v246\n"
    "v_accvgpr_write_b32 a175, v247\n"
    "v_lshlrev_b32 v244, 16, v126\n"
    "v_and_b32 v245, 0xffff0000, v126\n"
    "v_lshlrev_b32 v246, 16, v127\n"
    "v_and_b32 v247, 0xffff0000, v127\n"
    "v_accvgpr_write_b32 a176, v244\n"
    "v_accvgpr_write_b32 a177, v245\n"
    "v_accvgpr_write_b32 a178, v246\n"
    "v_accvgpr_write_b32 a179, v247\n"
    "v_accvgpr_write_b32 a180, v244\n"
    "v_accvgpr_write_b32 a181, v245\n"
    "v_accvgpr_write_b32 a182, v246\n"
    "v_accvgpr_write_b32 a183, v247\n"
    "v_accvgpr_write_b32 a184, v244\n"
    "v_accvgpr_write_b32 a185, v245\n"
    "v_accvgpr_write_b32 a186, v246\n"
    "v_accvgpr_write_b32 a187, v247\n"
    "v_accvgpr_write_b32 a188, v244\n"
    "v_accvgpr_write_b32 a189, v245\n"
    "v_accvgpr_write_b32 a190, v246\n"
    "v_accvgpr_write_b32 a191, v247\n"
    "v_lshlrev_b32 v244, 16, v128\n"
    "v_and_b32 v245, 0xffff0000, v128\n"
    "v_lshlrev_b32 v246, 16, v129\n"
    "v_and_b32 v247, 0xffff0000, v129\n"
    "v_accvgpr_write_b32 a192, v244\n"
    "v_accvgpr_write_b32 a193, v245\n"
    "v_accvgpr_write_b32 a194, v246\n"
    "v_accvgpr_write_b32 a195, v247\n"
    "v_accvgpr_write_b32 a196, v244\n"
    "v_accvgpr_write_b32 a197, v245\n"
    "v_accvgpr_write_b32 a198, v246\n"
    "v_accvgpr_write_b32 a199, v247\n"
    "v_accvgpr_write_b32 a200, v244\n"
    "v_accvgpr_write_b32 a201, v245\n"
    "v_accvgpr_write_b32 a202, v246\n"
    "v_accvgpr_write_b32 a203, v247\n"
    "v_accvgpr_write_b32 a204, v244\n"
    "v_accvgpr_write_b32 a205, v245\n"
    "v_accvgpr_write_b32 a206, v246\n"
    "v_accvgpr_write_b32 a207, v247\n"
    "v_and_b32 v246, 15, v240\n"
    "v_mul_u32_u24 v242, 1680, v246\n"
    "v_lshrrev_b32 v247, 4, v240\n"
    "v_lshlrev_b32 v247, 4, v247\n"
    "v_add_u32 v242, v242, v247\n"
    "v_add_u32 v243, 53760, v242\n"
    "ds_read_b128 v[104:107], v242 offset:0\n"
    "ds_read_b128 v[108:111], v242 offset:26880\n"
    "ds_read_b128 v[112:115], v243 offset:0\n"
    "ds_read_b128 v[116:119], v243 offset:26880\n"
    "s_mov_b32 s57, 13\n"
    "L_loop_1_%=:\n"
    "s_add_u32 s42, s40, 4096\n"
    "s_addc_u32 s43, s41, 0\n"
    "s_add_u32 s44, s40, 12288\n"
    "s_addc_u32 s45, s41, 0\n"
    "s_add_u32 s46, s40, 20480\n"
    "s_addc_u32 s47, s41, 0\n"
    "s_add_u32 s48, s40, 28672\n"
    "s_addc_u32 s49, s41, 0\n"
    "s_add_u32 s40, s40, 26624\n"
    "s_addc_u32 s41, s41, 0\n"
    "ds_read_b128 v[120:123], v242 offset:64\n"
    "ds_read_b128 v[124:127], v242 offset:26944\n"
    "ds_read_b128 v[128:131], v243 offset:64\n"
    "ds_read_b128 v[132:135], v243 offset:26944\n"
    "s_waitcnt vmcnt(25) lgkmcnt(4)\n"
    "v_mfma_f32_16x16x32_bf16 a[0:3], v[0:3], v[104:107], a[0:3]\n"
    "v_mfma_f32_16x16x32_bf16 a[4:7], v[0:3], v[108:111], a[4:7]\n"
    "v_mfma_f32_16x16x32_bf16 a[8:11], v[0:3], v[112:115], a[8:11]\n"
    "v_mfma_f32_16x16x32_bf16 a[12:15], v[0:3], v[116:119], a[12:15]\n"
    "global_load_dwordx4 v[0:3], v241, s[42:43] offset:-4096\n"
    "s_waitcnt vmcnt(25)\n"
    "v_mfma_f32_16x16x32_bf16 a[16:19], v[4:7], v[104:107], a[16:19]\n"
    "v_mfma_f32_16x16x32_bf16 a[20:23], v[4:7], v[108:111], a[20:23]\n"
    "v_mfma_f32_16x16x32_bf16 a[24:27], v[4:7], v[112:115], a[24:27]\n"
    "v_mfma_f32_16x16x32_bf16 a[28:31], v[4:7], v[116:119], a[28:31]\n"
    "global_load_dwordx4 v[4:7], v241, s[42:43] offset:-3072\n"
    "s_waitcnt vmcnt(25)\n"
    "v_mfma_f32_16x16x32_bf16 a[32:35], v[8:11], v[104:107], a[32:35]\n"
    "v_mfma_f32_16x16x32_bf16 a[36:39], v[8:11], v[108:111], a[36:39]\n"
    "v_mfma_f32_16x16x32_bf16 a[40:43], v[8:11], v[112:115], a[40:43]\n"
    "v_mfma_f32_16x16x32_bf16 a[44:47], v[8:11], v[116:119], a[44:47]\n"
    "global_load_dwordx4 v[8:11], v241, s[42:43] offset:-2048\n"
    "s_waitcnt vmcnt(25)\n"
    "v_mfma_f32_16x16x32_bf16 a[48:51], v[12:15], v[104:107], a[48:51]\n"
    "v_mfma_f32_16x16x32_bf16 a[52:55], v[12:15], v[108:111], a[52:55]\n"
    "v_mfma_f32_16x16x32_bf16 a[56:59], v[12:15], v[112:115], a[56:59]\n"
    "v_mfma_f32_16x16x32_bf16 a[60:63], v[12:15], v[116:119], a[60:63]\n"
    "global_load_dwordx4 v[12:15], v241, s[42:43] offset:-1024\n"
    "s_waitcnt vmcnt(25)\n"
    "v_mfma_f32_16x16x32_bf16 a[64:67], v[16:19], v[104:107], a[64:67]\n"
    "v_mfma_f32_16x16x32_bf16 a[68:71], v[16:19], v[108:111], a[68:71]\n"
    "v_mfma_f32_16x16x32_bf16 a[72:75], v[16:19], v[112:115], a[72:75]\n"
    "v_mfma_f32_16x16x32_bf16 a[76:79], v[16:19], v[116:119], a[76:79]\n"
    "global_load_dwordx4 v[16:19], v241, s[42:43] offset:0\n"
    "s_waitcnt vmcnt(25)\n"
    "v_mfma_f32_16x16x32_bf16 a[80:83], v[20:23], v[104:107], a[80:83]\n"
    "v_mfma_f32_16x16x32_bf16 a[84:87], v[20:23], v[108:111], a[84:87]\n"
    "v_mfma_f32_16x16x32_bf16 a[88:91], v[20:23], v[112:115], a[88:91]\n"
    "v_mfma_f32_16x16x32_bf16 a[92:95], v[20:23], v[116:119], a[92:95]\n"
    "global_load_dwordx4 v[20:23], v241, s[42:43] offset:1024\n"
    "s_waitcnt vmcnt(25)\n"
    "v_mfma_f32_16x16x32_bf16 a[96:99], v[24:27], v[104:107], a[96:99]\n"
    "v_mfma_f32_16x16x32_bf16 a[100:103], v[24:27], v[108:111], a[100:103]\n"
    "v_mfma_f32_16x16x32_bf16 a[104:107], v[24:27], v[112:115], a[104:107]\n"
    "v_mfma_f32_16x16x32_bf16 a[108:111], v[24:27], v[116:119], a[108:111]\n"
    "global_load_dwordx4 v[24:27], v241, s[42:43] offset:2048\n"
    "s_waitcnt vmcnt(25)\n"
    "v_mfma_f32_16x16x32_bf16 a[112:115], v[28:31], v[104:107], a[112:115]\n"
    "v_mfma_f32_16x16x32_bf16 a[116:119], v[28:31], v[108:111], a[116:119]\n"
    "v_mfma_f32_16x16x32_bf16 a[120:123], v[28:31], v[112:115], a[120:123]\n"
    "v_mfma_f32_16x16x32_bf16 a[124:127], v[28:31], v[116:119], a[124:127]\n"
    "global_load_dwordx4 v[28:31], v241, s[42:43] offset:3072\n"
    "s_waitcnt vmcnt(25)\n"
    "v_mfma_f32_16x16x32_bf16 a[128:131], v[32:35], v[104:107], a[128:131]\n"
    "v_mfma_f32_16x16x32_bf16 a[132:135], v[32:35], v[108:111], a[132:135]\n"
    "v_mfma_f32_16x16x32_bf16 a[136:139], v[32:35], v[112:115], a[136:139]\n"
    "v_mfma_f32_16x16x32_bf16 a[140:143], v[32:35], v[116:119], a[140:143]\n"
    "global_load_dwordx4 v[32:35], v241, s[44:45] offset:-4096\n"
    "s_waitcnt vmcnt(25)\n"
    "v_mfma_f32_16x16x32_bf16 a[144:147], v[36:39], v[104:107], a[144:147]\n"
    "v_mfma_f32_16x16x32_bf16 a[148:151], v[36:39], v[108:111], a[148:151]\n"
    "v_mfma_f32_16x16x32_bf16 a[152:155], v[36:39], v[112:115], a[152:155]\n"
    "v_mfma_f32_16x16x32_bf16 a[156:159], v[36:39], v[116:119], a[156:159]\n"
    "global_load_dwordx4 v[36:39], v241, s[44:45] offset:-3072\n"
    "s_waitcnt vmcnt(25)\n"
    "v_mfma_f32_16x16x32_bf16 a[160:163], v[40:43], v[104:107], a[160:163]\n"
    "v_mfma_f32_16x16x32_bf16 a[164:167], v[40:43], v[108:111], a[164:167]\n"
    "v_mfma_f32_16x16x32_bf16 a[168:171], v[40:43], v[112:115], a[168:171]\n"
    "v_mfma_f32_16x16x32_bf16 a[172:175], v[40:43], v[116:119], a[172:175]\n"
    "global_load_dwordx4 v[40:43], v241, s[44:45] offset:-2048\n"
    "s_waitcnt vmcnt(25)\n"
    "v_mfma_f32_16x16x32_bf16 a[176:179], v[44:47], v[104:107], a[176:179]\n"
    "v_mfma_f32_16x16x32_bf16 a[180:183], v[44:47], v[108:111], a[180:183]\n"
    "v_mfma_f32_16x16x32_bf16 a[184:187], v[44:47], v[112:115], a[184:187]\n"
    "v_mfma_f32_16x16x32_bf16 a[188:191], v[44:47], v[116:119], a[188:191]\n"
    "global_load_dwordx4 v[44:47], v241, s[44:45] offset:-1024\n"
    "s_waitcnt vmcnt(25)\n"
    "v_mfma_f32_16x16x32_bf16 a[192:195], v[48:51], v[104:107], a[192:195]\n"
    "v_mfma_f32_16x16x32_bf16 a[196:199], v[48:51], v[108:111], a[196:199]\n"
    "v_mfma_f32_16x16x32_bf16 a[200:203], v[48:51], v[112:115], a[200:203]\n"
    "v_mfma_f32_16x16x32_bf16 a[204:207], v[48:51], v[116:119], a[204:207]\n"
    "global_load_dwordx4 v[48:51], v241, s[44:45] offset:0\n"
    "ds_read_b128 v[104:107], v242 offset:128\n"
    "ds_read_b128 v[108:111], v242 offset:27008\n"
    "ds_read_b128 v[112:115], v243 offset:128\n"
    "ds_read_b128 v[116:119], v243 offset:27008\n"
    "s_waitcnt vmcnt(25) lgkmcnt(4)\n"
    "v_mfma_f32_16x16x32_bf16 a[0:3], v[52:55], v[120:123], a[0:3]\n"
    "v_mfma_f32_16x16x32_bf16 a[4:7], v[52:55], v[124:127], a[4:7]\n"
    "v_mfma_f32_16x16x32_bf16 a[8:11], v[52:55], v[128:131], a[8:11]\n"
    "v_mfma_f32_16x16x32_bf16 a[12:15], v[52:55], v[132:135], a[12:15]\n"
    "global_load_dwordx4 v[52:55], v241, s[44:45] offset:1024\n"
    "s_waitcnt vmcnt(25)\n"
    "v_mfma_f32_16x16x32_bf16 a[16:19], v[56:59], v[120:123], a[16:19]\n"
    "v_mfma_f32_16x16x32_bf16 a[20:23], v[56:59], v[124:127], a[20:23]\n"
    "v_mfma_f32_16x16x32_bf16 a[24:27], v[56:59], v[128:131], a[24:27]\n"
    "v_mfma_f32_16x16x32_bf16 a[28:31], v[56:59], v[132:135], a[28:31]\n"
    "global_load_dwordx4 v[56:59], v241, s[44:45] offset:2048\n"
    "s_waitcnt vmcnt(25)\n"
    "v_mfma_f32_16x16x32_bf16 a[32:35], v[60:63], v[120:123], a[32:35]\n"
    "v_mfma_f32_16x16x32_bf16 a[36:39], v[60:63], v[124:127], a[36:39]\n"
    "v_mfma_f32_16x16x32_bf16 a[40:43], v[60:63], v[128:131], a[40:43]\n"
    "v_mfma_f32_16x16x32_bf16 a[44:47], v[60:63], v[132:135], a[44:47]\n"
    "global_load_dwordx4 v[60:63], v241, s[44:45] offset:3072\n"
    "s_waitcnt vmcnt(25)\n"
    "v_mfma_f32_16x16x32_bf16 a[48:51], v[64:67], v[120:123], a[48:51]\n"
    "v_mfma_f32_16x16x32_bf16 a[52:55], v[64:67], v[124:127], a[52:55]\n"
    "v_mfma_f32_16x16x32_bf16 a[56:59], v[64:67], v[128:131], a[56:59]\n"
    "v_mfma_f32_16x16x32_bf16 a[60:63], v[64:67], v[132:135], a[60:63]\n"
    "global_load_dwordx4 v[64:67], v241, s[46:47] offset:-4096\n"
    "s_waitcnt vmcnt(25)\n"
    "v_mfma_f32_16x16x32_bf16 a[64:67], v[68:71], v[120:123], a[64:67]\n"
    "v_mfma_f32_16x16x32_bf16 a[68:71], v[68:71], v[124:127], a[68:71]\n"
    "v_mfma_f32_16x16x32_bf16 a[72:75], v[68:71], v[128:131], a[72:75]\n"
    "v_mfma_f32_16x16x32_bf16 a[76:79], v[68:71], v[132:135], a[76:79]\n"
    "global_load_dwordx4 v[68:71], v241, s[46:47] offset:-3072\n"
    "s_waitcnt vmcnt(25)\n"
    "v_mfma_f32_16x16x32_bf16 a[80:83], v[72:75], v[120:123], a[80:83]\n"
    "v_mfma_f32_16x16x32_bf16 a[84:87], v[72:75], v[124:127], a[84:87]\n"
    "v_mfma_f32_16x16x32_bf16 a[88:91], v[72:75], v[128:131], a[88:91]\n"
    "v_mfma_f32_16x16x32_bf16 a[92:95], v[72:75], v[132:135], a[92:95]\n"
    "global_load_dwordx4 v[72:75], v241, s[46:47] offset:-2048\n"
    "s_waitcnt vmcnt(25)\n"
    "v_mfma_f32_16x16x32_bf16 a[96:99], v[76:79], v[120:123], a[96:99]\n"
    "v_mfma_f32_16x16x32_bf16 a[100:103], v[76:79], v[124:127], a[100:103]\n"
    "v_mfma_f32_16x16x32_bf16 a[104:107], v[76:79], v[128:131], a[104:107]\n"
    "v_mfma_f32_16x16x32_bf16 a[108:111], v[76:79], v[132:135], a[108:111]\n"
    "global_load_dwordx4 v[76:79], v241, s[46:47] offset:-1024\n"
    "s_waitcnt vmcnt(25)\n"
    "v_mfma_f32_16x16x32_bf16 a[112:115], v[80:83], v[120:123], a[112:115]\n"
    "v_mfma_f32_16x16x32_bf16 a[116:119], v[80:83], v[124:127], a[116:119]\n"
    "v_mfma_f32_16x16x32_bf16 a[120:123], v[80:83], v[128:131], a[120:123]\n"
    "v_mfma_f32_16x16x32_bf16 a[124:127], v[80:83], v[132:135], a[124:127]\n"
    "global_load_dwordx4 v[80:83], v241, s[46:47] offset:0\n"
    "s_waitcnt vmcnt(25)\n"
    "v_mfma_f32_16x16x32_bf16 a[128:131], v[84:87], v[120:123], a[128:131]\n"
    "v_mfma_f32_16x16x32_bf16 a[132:135], v[84:87], v[124:127], a[132:135]\n"
    "v_mfma_f32_16x16x32_bf16 a[136:139], v[84:87], v[128:131], a[136:139]\n"
    "v_mfma_f32_16x16x32_bf16 a[140:143], v[84:87], v[132:135], a[140:143]\n"
    "global_load_dwordx4 v[84:87], v241, s[46:47] offset:1024\n"
    "s_waitcnt vmcnt(25)\n"
    "v_mfma_f32_16x16x32_bf16 a[144:147], v[88:91], v[120:123], a[144:147]\n"
    "v_mfma_f32_16x16x32_bf16 a[148:151], v[88:91], v[124:127], a[148:151]\n"
    "v_mfma_f32_16x16x32_bf16 a[152:155], v[88:91], v[128:131], a[152:155]\n"
    "v_mfma_f32_16x16x32_bf16 a[156:159], v[88:91], v[132:135], a[156:159]\n"
    "global_load_dwordx4 v[88:91], v241, s[46:47] offset:2048\n"
    "s_waitcnt vmcnt(25)\n"
    "v_mfma_f32_16x16x32_bf16 a[160:163], v[92:95], v[120:123], a[160:163]\n"
    "v_mfma_f32_16x16x32_bf16 a[164:167], v[92:95], v[124:127], a[164:167]\n"
    "v_mfma_f32_16x16x32_bf16 a[168:171], v[92:95], v[128:131], a[168:171]\n"
    "v_mfma_f32_16x16x32_bf16 a[172:175], v[92:95], v[132:135], a[172:175]\n"
    "global_load_dwordx4 v[92:95], v241, s[46:47] offset:3072\n"
    "s_waitcnt vmcnt(25)\n"
    "v_mfma_f32_16x16x32_bf16 a[176:179], v[96:99], v[120:123], a[176:179]\n"
    "v_mfma_f32_16x16x32_bf16 a[180:183], v[96:99], v[124:127], a[180:183]\n"
    "v_mfma_f32_16x16x32_bf16 a[184:187], v[96:99], v[128:131], a[184:187]\n"
    "v_mfma_f32_16x16x32_bf16 a[188:191], v[96:99], v[132:135], a[188:191]\n"
    "global_load_dwordx4 v[96:99], v241, s[48:49] offset:-4096\n"
    "s_waitcnt vmcnt(25)\n"
    "v_mfma_f32_16x16x32_bf16 a[192:195], v[100:103], v[120:123], a[192:195]\n"
    "v_mfma_f32_16x16x32_bf16 a[196:199], v[100:103], v[124:127], a[196:199]\n"
    "v_mfma_f32_16x16x32_bf16 a[200:203], v[100:103], v[128:131], a[200:203]\n"
    "v_mfma_f32_16x16x32_bf16 a[204:207], v[100:103], v[132:135], a[204:207]\n"
    "global_load_dwordx4 v[100:103], v241, s[48:49] offset:-3072\n"
    "v_add_u32 v242, 128, v242\n"
    "v_add_u32 v243, 128, v243\n"
    "s_sub_u32 s57, s57, 1\n"
    "s_cmp_lg_u32 s57, 0\n"
    "s_cbranch_scc1 L_loop_1_%=\n"
    "s_waitcnt lgkmcnt(0)\n"
    "s_nop 7\n"
    "s_nop 7\n"
    "s_barrier\n"
    "v_lshrrev_b32 v253, 4, v240\n"
    "v_lshlrev_b32 v253, 3, v253\n"
    "s_mul_i32 s58, s56, 416\n"
    "v_add_u32 v253, s58, v253\n"
    "v_and_b32 v254, 15, v240\n"
    "v_mul_u32_u24 v254, 1680, v254\n"
    "v_add_u32 v254, v254, v253\n"
    "v_add_u32 v255, 53760, v254\n"
    "ds_read_b64 v[104:105], v254 offset:0\n"
    "ds_read_b64 v[106:107], v254 offset:32\n"
    "ds_read_b64 v[108:109], v254 offset:64\n"
    "ds_read_b64 v[110:111], v254 offset:96\n"
    "ds_read_b64 v[112:113], v254 offset:128\n"
    "ds_read_b64 v[114:115], v254 offset:160\n"
    "ds_read_b64 v[116:117], v254 offset:192\n"
    "ds_read_b64 v[118:119], v254 offset:224\n"
    "ds_read_b64 v[120:121], v254 offset:256\n"
    "ds_read_b64 v[122:123], v254 offset:288\n"
    "ds_read_b64 v[124:125], v254 offset:320\n"
    "ds_read_b64 v[126:127], v254 offset:352\n"
    "ds_read_b64 v[128:129], v254 offset:384\n"
    "s_waitcnt lgkmcnt(12)\n"
    "v_accvgpr_read_b32 v244, a0\n"
    "v_accvgpr_read_b32 v245, a1\n"
    "v_accvgpr_read_b32 v246, a2\n"
    "v_accvgpr_read_b32 v247, a3\n"
    "v_lshlrev_b32 v248, 16, v136\n"
    "v_lshlrev_b32 v249, 16, v104\n"
    "v_fmac_f32 v249, v248, v244\n"
    "v_and_b32 v248, 0xffff0000, v136\n"
    "v_and_b32 v250, 0xffff0000, v104\n"
    "v_fmac_f32 v250, v248, v245\n"
    "v_cvt_pk_bf16_f32 v104, v249, v250\n"
    "v_lshlrev_b32 v248, 16, v137\n"
    "v_lshlrev_b32 v249, 16, v105\n"
    "v_fmac_f32 v249, v248, v246\n"
    "v_and_b32 v248, 0xffff0000, v137\n"
    "v_and_b32 v250, 0xffff0000, v105\n"
    "v_fmac_f32 v250, v248, v247\n"
    "v_cvt_pk_bf16_f32 v105, v249, v250\n"
    "s_waitcnt lgkmcnt(11)\n"
    "v_accvgpr_read_b32 v244, a16\n"
    "v_accvgpr_read_b32 v245, a17\n"
    "v_accvgpr_read_b32 v246, a18\n"
    "v_accvgpr_read_b32 v247, a19\n"
    "v_lshlrev_b32 v248, 16, v138\n"
    "v_lshlrev_b32 v249, 16, v106\n"
    "v_fmac_f32 v249, v248, v244\n"
    "v_and_b32 v248, 0xffff0000, v138\n"
    "v_and_b32 v250, 0xffff0000, v106\n"
    "v_fmac_f32 v250, v248, v245\n"
    "v_cvt_pk_bf16_f32 v106, v249, v250\n"
    "v_lshlrev_b32 v248, 16, v139\n"
    "v_lshlrev_b32 v249, 16, v107\n"
    "v_fmac_f32 v249, v248, v246\n"
    "v_and_b32 v248, 0xffff0000, v139\n"
    "v_and_b32 v250, 0xffff0000, v107\n"
    "v_fmac_f32 v250, v248, v247\n"
    "v_cvt_pk_bf16_f32 v107, v249, v250\n"
    "s_waitcnt lgkmcnt(10)\n"
    "v_accvgpr_read_b32 v244, a32\n"
    "v_accvgpr_read_b32 v245, a33\n"
    "v_accvgpr_read_b32 v246, a34\n"
    "v_accvgpr_read_b32 v247, a35\n"
    "v_lshlrev_b32 v248, 16, v140\n"
    "v_lshlrev_b32 v249, 16, v108\n"
    "v_fmac_f32 v249, v248, v244\n"
    "v_and_b32 v248, 0xffff0000, v140\n"
    "v_and_b32 v250, 0xffff0000, v108\n"
    "v_fmac_f32 v250, v248, v245\n"
    "v_cvt_pk_bf16_f32 v108, v249, v250\n"
    "v_lshlrev_b32 v248, 16, v141\n"
    "v_lshlrev_b32 v249, 16, v109\n"
    "v_fmac_f32 v249, v248, v246\n"
    "v_and_b32 v248, 0xffff0000, v141\n"
    "v_and_b32 v250, 0xffff0000, v109\n"
    "v_fmac_f32 v250, v248, v247\n"
    "v_cvt_pk_bf16_f32 v109, v249, v250\n"
    "s_waitcnt lgkmcnt(9)\n"
    "v_accvgpr_read_b32 v244, a48\n"
    "v_accvgpr_read_b32 v245, a49\n"
    "v_accvgpr_read_b32 v246, a50\n"
    "v_accvgpr_read_b32 v247, a51\n"
    "v_lshlrev_b32 v248, 16, v142\n"
    "v_lshlrev_b32 v249, 16, v110\n"
    "v_fmac_f32 v249, v248, v244\n"
    "v_and_b32 v248, 0xffff0000, v142\n"
    "v_and_b32 v250, 0xffff0000, v110\n"
    "v_fmac_f32 v250, v248, v245\n"
    "v_cvt_pk_bf16_f32 v110, v249, v250\n"
    "v_lshlrev_b32 v248, 16, v143\n"
    "v_lshlrev_b32 v249, 16, v111\n"
    "v_fmac_f32 v249, v248, v246\n"
    "v_and_b32 v248, 0xffff0000, v143\n"
    "v_and_b32 v250, 0xffff0000, v111\n"
    "v_fmac_f32 v250, v248, v247\n"
    "v_cvt_pk_bf16_f32 v111, v249, v250\n"
    "s_waitcnt lgkmcnt(8)\n"
    "v_accvgpr_read_b32 v244, a64\n"
    "v_accvgpr_read_b32 v245, a65\n"
    "v_accvgpr_read_b32 v246, a66\n"
    "v_accvgpr_read_b32 v247, a67\n"
    "v_lshlrev_b32 v248, 16, v144\n"
    "v_lshlrev_b32 v249, 16, v112\n"
    "v_fmac_f32 v249, v248, v244\n"
    "v_and_b32 v248, 0xffff0000, v144\n"
    "v_and_b32 v250, 0xffff0000, v112\n"
    "v_fmac_f32 v250, v248, v245\n"
    "v_cvt_pk_bf16_f32 v112, v249, v250\n"
    "v_lshlrev_b32 v248, 16, v145\n"
    "v_lshlrev_b32 v249, 16, v113\n"
    "v_fmac_f32 v249, v248, v246\n"
    "v_and_b32 v248, 0xffff0000, v145\n"
    "v_and_b32 v250, 0xffff0000, v113\n"
    "v_fmac_f32 v250, v248, v247\n"
    "v_cvt_pk_bf16_f32 v113, v249, v250\n"
    "s_waitcnt lgkmcnt(7)\n"
    "v_accvgpr_read_b32 v244, a80\n"
    "v_accvgpr_read_b32 v245, a81\n"
    "v_accvgpr_read_b32 v246, a82\n"
    "v_accvgpr_read_b32 v247, a83\n"
    "v_lshlrev_b32 v248, 16, v146\n"
    "v_lshlrev_b32 v249, 16, v114\n"
    "v_fmac_f32 v249, v248, v244\n"
    "v_and_b32 v248, 0xffff0000, v146\n"
    "v_and_b32 v250, 0xffff0000, v114\n"
    "v_fmac_f32 v250, v248, v245\n"
    "v_cvt_pk_bf16_f32 v114, v249, v250\n"
    "v_lshlrev_b32 v248, 16, v147\n"
    "v_lshlrev_b32 v249, 16, v115\n"
    "v_fmac_f32 v249, v248, v246\n"
    "v_and_b32 v248, 0xffff0000, v147\n"
    "v_and_b32 v250, 0xffff0000, v115\n"
    "v_fmac_f32 v250, v248, v247\n"
    "v_cvt_pk_bf16_f32 v115, v249, v250\n"
    "s_waitcnt lgkmcnt(6)\n"
    "v_accvgpr_read_b32 v244, a96\n"
    "v_accvgpr_read_b32 v245, a97\n"
    "v_accvgpr_read_b32 v246, a98\n"
    "v_accvgpr_read_b32 v247, a99\n"
    "v_lshlrev_b32 v248, 16, v148\n"
    "v_lshlrev_b32 v249, 16, v116\n"
    "v_fmac_f32 v249, v248, v244\n"
    "v_and_b32 v248, 0xffff0000, v148\n"
    "v_and_b32 v250, 0xffff0000, v116\n"
    "v_fmac_f32 v250, v248, v245\n"
    "v_cvt_pk_bf16_f32 v116, v249, v250\n"
    "v_lshlrev_b32 v248, 16, v149\n"
    "v_lshlrev_b32 v249, 16, v117\n"
    "v_fmac_f32 v249, v248, v246\n"
    "v_and_b32 v248, 0xffff0000, v149\n"
    "v_and_b32 v250, 0xffff0000, v117\n"
    "v_fmac_f32 v250, v248, v247\n"
    "v_cvt_pk_bf16_f32 v117, v249, v250\n"
    "s_waitcnt lgkmcnt(5)\n"
    "v_accvgpr_read_b32 v244, a112\n"
    "v_accvgpr_read_b32 v245, a113\n"
    "v_accvgpr_read_b32 v246, a114\n"
    "v_accvgpr_read_b32 v247, a115\n"
    "v_lshlrev_b32 v248, 16, v150\n"
    "v_lshlrev_b32 v249, 16, v118\n"
    "v_fmac_f32 v249, v248, v244\n"
    "v_and_b32 v248, 0xffff0000, v150\n"
    "v_and_b32 v250, 0xffff0000, v118\n"
    "v_fmac_f32 v250, v248, v245\n"
    "v_cvt_pk_bf16_f32 v118, v249, v250\n"
    "v_lshlrev_b32 v248, 16, v151\n"
    "v_lshlrev_b32 v249, 16, v119\n"
    "v_fmac_f32 v249, v248, v246\n"
    "v_and_b32 v248, 0xffff0000, v151\n"
    "v_and_b32 v250, 0xffff0000, v119\n"
    "v_fmac_f32 v250, v248, v247\n"
    "v_cvt_pk_bf16_f32 v119, v249, v250\n"
    "s_waitcnt lgkmcnt(4)\n"
    "v_accvgpr_read_b32 v244, a128\n"
    "v_accvgpr_read_b32 v245, a129\n"
    "v_accvgpr_read_b32 v246, a130\n"
    "v_accvgpr_read_b32 v247, a131\n"
    "v_lshlrev_b32 v248, 16, v152\n"
    "v_lshlrev_b32 v249, 16, v120\n"
    "v_fmac_f32 v249, v248, v244\n"
    "v_and_b32 v248, 0xffff0000, v152\n"
    "v_and_b32 v250, 0xffff0000, v120\n"
    "v_fmac_f32 v250, v248, v245\n"
    "v_cvt_pk_bf16_f32 v120, v249, v250\n"
    "v_lshlrev_b32 v248, 16, v153\n"
    "v_lshlrev_b32 v249, 16, v121\n"
    "v_fmac_f32 v249, v248, v246\n"
    "v_and_b32 v248, 0xffff0000, v153\n"
    "v_and_b32 v250, 0xffff0000, v121\n"
    "v_fmac_f32 v250, v248, v247\n"
    "v_cvt_pk_bf16_f32 v121, v249, v250\n"
    "s_waitcnt lgkmcnt(3)\n"
    "v_accvgpr_read_b32 v244, a144\n"
    "v_accvgpr_read_b32 v245, a145\n"
    "v_accvgpr_read_b32 v246, a146\n"
    "v_accvgpr_read_b32 v247, a147\n"
    "v_lshlrev_b32 v248, 16, v154\n"
    "v_lshlrev_b32 v249, 16, v122\n"
    "v_fmac_f32 v249, v248, v244\n"
    "v_and_b32 v248, 0xffff0000, v154\n"
    "v_and_b32 v250, 0xffff0000, v122\n"
    "v_fmac_f32 v250, v248, v245\n"
    "v_cvt_pk_bf16_f32 v122, v249, v250\n"
    "v_lshlrev_b32 v248, 16, v155\n"
    "v_lshlrev_b32 v249, 16, v123\n"
    "v_fmac_f32 v249, v248, v246\n"
    "v_and_b32 v248, 0xffff0000, v155\n"
    "v_and_b32 v250, 0xffff0000, v123\n"
    "v_fmac_f32 v250, v248, v247\n"
    "v_cvt_pk_bf16_f32 v123, v249, v250\n"
    "s_waitcnt lgkmcnt(2)\n"
    "v_accvgpr_read_b32 v244, a160\n"
    "v_accvgpr_read_b32 v245, a161\n"
    "v_accvgpr_read_b32 v246, a162\n"
    "v_accvgpr_read_b32 v247, a163\n"
    "v_lshlrev_b32 v248, 16, v156\n"
    "v_lshlrev_b32 v249, 16, v124\n"
    "v_fmac_f32 v249, v248, v244\n"
    "v_and_b32 v248, 0xffff0000, v156\n"
    "v_and_b32 v250, 0xffff0000, v124\n"
    "v_fmac_f32 v250, v248, v245\n"
    "v_cvt_pk_bf16_f32 v124, v249, v250\n"
    "v_lshlrev_b32 v248, 16, v157\n"
    "v_lshlrev_b32 v249, 16, v125\n"
    "v_fmac_f32 v249, v248, v246\n"
    "v_and_b32 v248, 0xffff0000, v157\n"
    "v_and_b32 v250, 0xffff0000, v125\n"
    "v_fmac_f32 v250, v248, v247\n"
    "v_cvt_pk_bf16_f32 v125, v249, v250\n"
    "s_waitcnt lgkmcnt(1)\n"
    "v_accvgpr_read_b32 v244, a176\n"
    "v_accvgpr_read_b32 v245, a177\n"
    "v_accvgpr_read_b32 v246, a178\n"
    "v_accvgpr_read_b32 v247, a179\n"
    "v_lshlrev_b32 v248, 16, v158\n"
    "v_lshlrev_b32 v249, 16, v126\n"
    "v_fmac_f32 v249, v248, v244\n"
    "v_and_b32 v248, 0xffff0000, v158\n"
    "v_and_b32 v250, 0xffff0000, v126\n"
    "v_fmac_f32 v250, v248, v245\n"
    "v_cvt_pk_bf16_f32 v126, v249, v250\n"
    "v_lshlrev_b32 v248, 16, v159\n"
    "v_lshlrev_b32 v249, 16, v127\n"
    "v_fmac_f32 v249, v248, v246\n"
    "v_and_b32 v248, 0xffff0000, v159\n"
    "v_and_b32 v250, 0xffff0000, v127\n"
    "v_fmac_f32 v250, v248, v247\n"
    "v_cvt_pk_bf16_f32 v127, v249, v250\n"
    "s_waitcnt lgkmcnt(0)\n"
    "v_accvgpr_read_b32 v244, a192\n"
    "v_accvgpr_read_b32 v245, a193\n"
    "v_accvgpr_read_b32 v246, a194\n"
    "v_accvgpr_read_b32 v247, a195\n"
    "v_lshlrev_b32 v248, 16, v160\n"
    "v_lshlrev_b32 v249, 16, v128\n"
    "v_fmac_f32 v249, v248, v244\n"
    "v_and_b32 v248, 0xffff0000, v160\n"
    "v_and_b32 v250, 0xffff0000, v128\n"
    "v_fmac_f32 v250, v248, v245\n"
    "v_cvt_pk_bf16_f32 v128, v249, v250\n"
    "v_lshlrev_b32 v248, 16, v161\n"
    "v_lshlrev_b32 v249, 16, v129\n"
    "v_fmac_f32 v249, v248, v246\n"
    "v_and_b32 v248, 0xffff0000, v161\n"
    "v_and_b32 v250, 0xffff0000, v129\n"
    "v_fmac_f32 v250, v248, v247\n"
    "v_cvt_pk_bf16_f32 v129, v249, v250\n"
    "ds_write_b64 v254, v[104:105] offset:0\n"
    "ds_write_b64 v254, v[106:107] offset:32\n"
    "ds_write_b64 v254, v[108:109] offset:64\n"
    "ds_write_b64 v254, v[110:111] offset:96\n"
    "ds_write_b64 v254, v[112:113] offset:128\n"
    "ds_write_b64 v254, v[114:115] offset:160\n"
    "ds_write_b64 v254, v[116:117] offset:192\n"
    "ds_write_b64 v254, v[118:119] offset:224\n"
    "ds_write_b64 v254, v[120:121] offset:256\n"
    "ds_write_b64 v254, v[122:123] offset:288\n"
    "ds_write_b64 v254, v[124:125] offset:320\n"
    "ds_write_b64 v254, v[126:127] offset:352\n"
    "ds_write_b64 v254, v[128:129] offset:384\n"
    "s_waitcnt lgkmcnt(0)\n"
    "ds_read_b64 v[104:105], v254 offset:26880\n"
    "ds_read_b64 v[106:107], v254 offset:26912\n"
    "ds_read_b64 v[108:109], v254 offset:26944\n"
    "ds_read_b64 v[110:111], v254 offset:26976\n"
    "ds_read_b64 v[112:113], v254 offset:27008\n"
    "ds_read_b64 v[114:115], v254 offset:27040\n"
    "ds_read_b64 v[116:117], v254 offset:27072\n"
    "ds_read_b64 v[118:119], v254 offset:27104\n"
    "ds_read_b64 v[120:121], v254 offset:27136\n"
    "ds_read_b64 v[122:123], v254 offset:27168\n"
    "ds_read_b64 v[124:125], v254 offset:27200\n"
    "ds_read_b64 v[126:127], v254 offset:27232\n"
    "ds_read_b64 v[128:129], v254 offset:27264\n"
    "s_waitcnt lgkmcnt(12)\n"
    "v_accvgpr_read_b32 v244, a4\n"
    "v_accvgpr_read_b32 v245, a5\n"
    "v_accvgpr_read_b32 v246, a6\n"
    "v_accvgpr_read_b32 v247, a7\n"
    "v_lshlrev_b32 v248, 16, v162\n"
    "v_lshlrev_b32 v249, 16, v104\n"
    "v_fmac_f32 v249, v248, v244\n"
    "v_and_b32 v248, 0xffff0000, v162\n"
    "v_and_b32 v250, 0xffff0000, v104\n"
    "v_fmac_f32 v250, v248, v245\n"
    "v_cvt_pk_bf16_f32 v104, v249, v250\n"
    "v_lshlrev_b32 v248, 16, v163\n"
    "v_lshlrev_b32 v249, 16, v105\n"
    "v_fmac_f32 v249, v248, v246\n"
    "v_and_b32 v248, 0xffff0000, v163\n"
    "v_and_b32 v250, 0xffff0000, v105\n"
    "v_fmac_f32 v250, v248, v247\n"
    "v_cvt_pk_bf16_f32 v105, v249, v250\n"
    "s_waitcnt lgkmcnt(11)\n"
    "v_accvgpr_read_b32 v244, a20\n"
    "v_accvgpr_read_b32 v245, a21\n"
    "v_accvgpr_read_b32 v246, a22\n"
    "v_accvgpr_read_b32 v247, a23\n"
    "v_lshlrev_b32 v248, 16, v164\n"
    "v_lshlrev_b32 v249, 16, v106\n"
    "v_fmac_f32 v249, v248, v244\n"
    "v_and_b32 v248, 0xffff0000, v164\n"
    "v_and_b32 v250, 0xffff0000, v106\n"
    "v_fmac_f32 v250, v248, v245\n"
    "v_cvt_pk_bf16_f32 v106, v249, v250\n"
    "v_lshlrev_b32 v248, 16, v165\n"
    "v_lshlrev_b32 v249, 16, v107\n"
    "v_fmac_f32 v249, v248, v246\n"
    "v_and_b32 v248, 0xffff0000, v165\n"
    "v_and_b32 v250, 0xffff0000, v107\n"
    "v_fmac_f32 v250, v248, v247\n"
    "v_cvt_pk_bf16_f32 v107, v249, v250\n"
    "s_waitcnt lgkmcnt(10)\n"
    "v_accvgpr_read_b32 v244, a36\n"
    "v_accvgpr_read_b32 v245, a37\n"
    "v_accvgpr_read_b32 v246, a38\n"
    "v_accvgpr_read_b32 v247, a39\n"
    "v_lshlrev_b32 v248, 16, v166\n"
    "v_lshlrev_b32 v249, 16, v108\n"
    "v_fmac_f32 v249, v248, v244\n"
    "v_and_b32 v248, 0xffff0000, v166\n"
    "v_and_b32 v250, 0xffff0000, v108\n"
    "v_fmac_f32 v250, v248, v245\n"
    "v_cvt_pk_bf16_f32 v108, v249, v250\n"
    "v_lshlrev_b32 v248, 16, v167\n"
    "v_lshlrev_b32 v249, 16, v109\n"
    "v_fmac_f32 v249, v248, v246\n"
    "v_and_b32 v248, 0xffff0000, v167\n"
    "v_and_b32 v250, 0xffff0000, v109\n"
    "v_fmac_f32 v250, v248, v247\n"
    "v_cvt_pk_bf16_f32 v109, v249, v250\n"
    "s_waitcnt lgkmcnt(9)\n"
    "v_accvgpr_read_b32 v244, a52\n"
    "v_accvgpr_read_b32 v245, a53\n"
    "v_accvgpr_read_b32 v246, a54\n"
    "v_accvgpr_read_b32 v247, a55\n"
    "v_lshlrev_b32 v248, 16, v168\n"
    "v_lshlrev_b32 v249, 16, v110\n"
    "v_fmac_f32 v249, v248, v244\n"
    "v_and_b32 v248, 0xffff0000, v168\n"
    "v_and_b32 v250, 0xffff0000, v110\n"
    "v_fmac_f32 v250, v248, v245\n"
    "v_cvt_pk_bf16_f32 v110, v249, v250\n"
    "v_lshlrev_b32 v248, 16, v169\n"
    "v_lshlrev_b32 v249, 16, v111\n"
    "v_fmac_f32 v249, v248, v246\n"
    "v_and_b32 v248, 0xffff0000, v169\n"
    "v_and_b32 v250, 0xffff0000, v111\n"
    "v_fmac_f32 v250, v248, v247\n"
    "v_cvt_pk_bf16_f32 v111, v249, v250\n"
    "s_waitcnt lgkmcnt(8)\n"
    "v_accvgpr_read_b32 v244, a68\n"
    "v_accvgpr_read_b32 v245, a69\n"
    "v_accvgpr_read_b32 v246, a70\n"
    "v_accvgpr_read_b32 v247, a71\n"
    "v_lshlrev_b32 v248, 16, v170\n"
    "v_lshlrev_b32 v249, 16, v112\n"
    "v_fmac_f32 v249, v248, v244\n"
    "v_and_b32 v248, 0xffff0000, v170\n"
    "v_and_b32 v250, 0xffff0000, v112\n"
    "v_fmac_f32 v250, v248, v245\n"
    "v_cvt_pk_bf16_f32 v112, v249, v250\n"
    "v_lshlrev_b32 v248, 16, v171\n"
    "v_lshlrev_b32 v249, 16, v113\n"
    "v_fmac_f32 v249, v248, v246\n"
    "v_and_b32 v248, 0xffff0000, v171\n"
    "v_and_b32 v250, 0xffff0000, v113\n"
    "v_fmac_f32 v250, v248, v247\n"
    "v_cvt_pk_bf16_f32 v113, v249, v250\n"
    "s_waitcnt lgkmcnt(7)\n"
    "v_accvgpr_read_b32 v244, a84\n"
    "v_accvgpr_read_b32 v245, a85\n"
    "v_accvgpr_read_b32 v246, a86\n"
    "v_accvgpr_read_b32 v247, a87\n"
    "v_lshlrev_b32 v248, 16, v172\n"
    "v_lshlrev_b32 v249, 16, v114\n"
    "v_fmac_f32 v249, v248, v244\n"
    "v_and_b32 v248, 0xffff0000, v172\n"
    "v_and_b32 v250, 0xffff0000, v114\n"
    "v_fmac_f32 v250, v248, v245\n"
    "v_cvt_pk_bf16_f32 v114, v249, v250\n"
    "v_lshlrev_b32 v248, 16, v173\n"
    "v_lshlrev_b32 v249, 16, v115\n"
    "v_fmac_f32 v249, v248, v246\n"
    "v_and_b32 v248, 0xffff0000, v173\n"
    "v_and_b32 v250, 0xffff0000, v115\n"
    "v_fmac_f32 v250, v248, v247\n"
    "v_cvt_pk_bf16_f32 v115, v249, v250\n"
    "s_waitcnt lgkmcnt(6)\n"
    "v_accvgpr_read_b32 v244, a100\n"
    "v_accvgpr_read_b32 v245, a101\n"
    "v_accvgpr_read_b32 v246, a102\n"
    "v_accvgpr_read_b32 v247, a103\n"
    "v_lshlrev_b32 v248, 16, v174\n"
    "v_lshlrev_b32 v249, 16, v116\n"
    "v_fmac_f32 v249, v248, v244\n"
    "v_and_b32 v248, 0xffff0000, v174\n"
    "v_and_b32 v250, 0xffff0000, v116\n"
    "v_fmac_f32 v250, v248, v245\n"
    "v_cvt_pk_bf16_f32 v116, v249, v250\n"
    "v_lshlrev_b32 v248, 16, v175\n"
    "v_lshlrev_b32 v249, 16, v117\n"
    "v_fmac_f32 v249, v248, v246\n"
    "v_and_b32 v248, 0xffff0000, v175\n"
    "v_and_b32 v250, 0xffff0000, v117\n"
    "v_fmac_f32 v250, v248, v247\n"
    "v_cvt_pk_bf16_f32 v117, v249, v250\n"
    "s_waitcnt lgkmcnt(5)\n"
    "v_accvgpr_read_b32 v244, a116\n"
    "v_accvgpr_read_b32 v245, a117\n"
    "v_accvgpr_read_b32 v246, a118\n"
    "v_accvgpr_read_b32 v247, a119\n"
    "v_lshlrev_b32 v248, 16, v176\n"
    "v_lshlrev_b32 v249, 16, v118\n"
    "v_fmac_f32 v249, v248, v244\n"
    "v_and_b32 v248, 0xffff0000, v176\n"
    "v_and_b32 v250, 0xffff0000, v118\n"
    "v_fmac_f32 v250, v248, v245\n"
    "v_cvt_pk_bf16_f32 v118, v249, v250\n"
    "v_lshlrev_b32 v248, 16, v177\n"
    "v_lshlrev_b32 v249, 16, v119\n"
    "v_fmac_f32 v249, v248, v246\n"
    "v_and_b32 v248, 0xffff0000, v177\n"
    "v_and_b32 v250, 0xffff0000, v119\n"
    "v_fmac_f32 v250, v248, v247\n"
    "v_cvt_pk_bf16_f32 v119, v249, v250\n"
    "s_waitcnt lgkmcnt(4)\n"
    "v_accvgpr_read_b32 v244, a132\n"
    "v_accvgpr_read_b32 v245, a133\n"
    "v_accvgpr_read_b32 v246, a134\n"
    "v_accvgpr_read_b32 v247, a135\n"
    "v_lshlrev_b32 v248, 16, v178\n"
    "v_lshlrev_b32 v249, 16, v120\n"
    "v_fmac_f32 v249, v248, v244\n"
    "v_and_b32 v248, 0xffff0000, v178\n"
    "v_and_b32 v250, 0xffff0000, v120\n"
    "v_fmac_f32 v250, v248, v245\n"
    "v_cvt_pk_bf16_f32 v120, v249, v250\n"
    "v_lshlrev_b32 v248, 16, v179\n"
    "v_lshlrev_b32 v249, 16, v121\n"
    "v_fmac_f32 v249, v248, v246\n"
    "v_and_b32 v248, 0xffff0000, v179\n"
    "v_and_b32 v250, 0xffff0000, v121\n"
    "v_fmac_f32 v250, v248, v247\n"
    "v_cvt_pk_bf16_f32 v121, v249, v250\n"
    "s_waitcnt lgkmcnt(3)\n"
    "v_accvgpr_read_b32 v244, a148\n"
    "v_accvgpr_read_b32 v245, a149\n"
    "v_accvgpr_read_b32 v246, a150\n"
    "v_accvgpr_read_b32 v247, a151\n"
    "v_lshlrev_b32 v248, 16, v180\n"
    "v_lshlrev_b32 v249, 16, v122\n"
    "v_fmac_f32 v249, v248, v244\n"
    "v_and_b32 v248, 0xffff0000, v180\n"
    "v_and_b32 v250, 0xffff0000, v122\n"
    "v_fmac_f32 v250, v248, v245\n"
    "v_cvt_pk_bf16_f32 v122, v249, v250\n"
    "v_lshlrev_b32 v248, 16, v181\n"
    "v_lshlrev_b32 v249, 16, v123\n"
    "v_fmac_f32 v249, v248, v246\n"
    "v_and_b32 v248, 0xffff0000, v181\n"
    "v_and_b32 v250, 0xffff0000, v123\n"
    "v_fmac_f32 v250, v248, v247\n"
    "v_cvt_pk_bf16_f32 v123, v249, v250\n"
    "s_waitcnt lgkmcnt(2)\n"
    "v_accvgpr_read_b32 v244, a164\n"
    "v_accvgpr_read_b32 v245, a165\n"
    "v_accvgpr_read_b32 v246, a166\n"
    "v_accvgpr_read_b32 v247, a167\n"
    "v_lshlrev_b32 v248, 16, v182\n"
    "v_lshlrev_b32 v249, 16, v124\n"
    "v_fmac_f32 v249, v248, v244\n"
    "v_and_b32 v248, 0xffff0000, v182\n"
    "v_and_b32 v250, 0xffff0000, v124\n"
    "v_fmac_f32 v250, v248, v245\n"
    "v_cvt_pk_bf16_f32 v124, v249, v250\n"
    "v_lshlrev_b32 v248, 16, v183\n"
    "v_lshlrev_b32 v249, 16, v125\n"
    "v_fmac_f32 v249, v248, v246\n"
    "v_and_b32 v248, 0xffff0000, v183\n"
    "v_and_b32 v250, 0xffff0000, v125\n"
    "v_fmac_f32 v250, v248, v247\n"
    "v_cvt_pk_bf16_f32 v125, v249, v250\n"
    "s_waitcnt lgkmcnt(1)\n"
    "v_accvgpr_read_b32 v244, a180\n"
    "v_accvgpr_read_b32 v245, a181\n"
    "v_accvgpr_read_b32 v246, a182\n"
    "v_accvgpr_read_b32 v247, a183\n"
    "v_lshlrev_b32 v248, 16, v184\n"
    "v_lshlrev_b32 v249, 16, v126\n"
    "v_fmac_f32 v249, v248, v244\n"
    "v_and_b32 v248, 0xffff0000, v184\n"
    "v_and_b32 v250, 0xffff0000, v126\n"
    "v_fmac_f32 v250, v248, v245\n"
    "v_cvt_pk_bf16_f32 v126, v249, v250\n"
    "v_lshlrev_b32 v248, 16, v185\n"
    "v_lshlrev_b32 v249, 16, v127\n"
    "v_fmac_f32 v249, v248, v246\n"
    "v_and_b32 v248, 0xffff0000, v185\n"
    "v_and_b32 v250, 0xffff0000, v127\n"
    "v_fmac_f32 v250, v248, v247\n"
    "v_cvt_pk_bf16_f32 v127, v249, v250\n"
    "s_waitcnt lgkmcnt(0)\n"
    "v_accvgpr_read_b32 v244, a196\n"
    "v_accvgpr_read_b32 v245, a197\n"
    "v_accvgpr_read_b32 v246, a198\n"
    "v_accvgpr_read_b32 v247, a199\n"
    "v_lshlrev_b32 v248, 16, v186\n"
    "v_lshlrev_b32 v249, 16, v128\n"
    "v_fmac_f32 v249, v248, v244\n"
    "v_and_b32 v248, 0xffff0000, v186\n"
    "v_and_b32 v250, 0xffff0000, v128\n"
    "v_fmac_f32 v250, v248, v245\n"
    "v_cvt_pk_bf16_f32 v128, v249, v250\n"
    "v_lshlrev_b32 v248, 16, v187\n"
    "v_lshlrev_b32 v249, 16, v129\n"
    "v_fmac_f32 v249, v248, v246\n"
    "v_and_b32 v248, 0xffff0000, v187\n"
    "v_and_b32 v250, 0xffff0000, v129\n"
    "v_fmac_f32 v250, v248, v247\n"
    "v_cvt_pk_bf16_f32 v129, v249, v250\n"
    "ds_write_b64 v254, v[104:105] offset:26880\n"
    "ds_write_b64 v254, v[106:107] offset:26912\n"
    "ds_write_b64 v254, v[108:109] offset:26944\n"
    "ds_write_b64 v254, v[110:111] offset:26976\n"
    "ds_write_b64 v254, v[112:113] offset:27008\n"
    "ds_write_b64 v254, v[114:115] offset:27040\n"
    "ds_write_b64 v254, v[116:117] offset:27072\n"
    "ds_write_b64 v254, v[118:119] offset:27104\n"
    "ds_write_b64 v254, v[120:121] offset:27136\n"
    "ds_write_b64 v254, v[122:123] offset:27168\n"
    "ds_write_b64 v254, v[124:125] offset:27200\n"
    "ds_write_b64 v254, v[126:127] offset:27232\n"
    "ds_write_b64 v254, v[128:129] offset:27264\n"
    "s_waitcnt lgkmcnt(0)\n"
    "ds_read_b64 v[104:105], v255 offset:0\n"
    "ds_read_b64 v[106:107], v255 offset:32\n"
    "ds_read_b64 v[108:109], v255 offset:64\n"
    "ds_read_b64 v[110:111], v255 offset:96\n"
    "ds_read_b64 v[112:113], v255 offset:128\n"
    "ds_read_b64 v[114:115], v255 offset:160\n"
    "ds_read_b64 v[116:117], v255 offset:192\n"
    "ds_read_b64 v[118:119], v255 offset:224\n"
    "ds_read_b64 v[120:121], v255 offset:256\n"
    "ds_read_b64 v[122:123], v255 offset:288\n"
    "ds_read_b64 v[124:125], v255 offset:320\n"
    "ds_read_b64 v[126:127], v255 offset:352\n"
    "ds_read_b64 v[128:129], v255 offset:384\n"
    "s_waitcnt lgkmcnt(12)\n"
    "v_accvgpr_read_b32 v244, a8\n"
    "v_accvgpr_read_b32 v245, a9\n"
    "v_accvgpr_read_b32 v246, a10\n"
    "v_accvgpr_read_b32 v247, a11\n"
    "v_lshlrev_b32 v248, 16, v188\n"
    "v_lshlrev_b32 v249, 16, v104\n"
    "v_fmac_f32 v249, v248, v244\n"
    "v_and_b32 v248, 0xffff0000, v188\n"
    "v_and_b32 v250, 0xffff0000, v104\n"
    "v_fmac_f32 v250, v248, v245\n"
    "v_cvt_pk_bf16_f32 v104, v249, v250\n"
    "v_lshlrev_b32 v248, 16, v189\n"
    "v_lshlrev_b32 v249, 16, v105\n"
    "v_fmac_f32 v249, v248, v246\n"
    "v_and_b32 v248, 0xffff0000, v189\n"
    "v_and_b32 v250, 0xffff0000, v105\n"
    "v_fmac_f32 v250, v248, v247\n"
    "v_cvt_pk_bf16_f32 v105, v249, v250\n"
    "s_waitcnt lgkmcnt(11)\n"
    "v_accvgpr_read_b32 v244, a24\n"
    "v_accvgpr_read_b32 v245, a25\n"
    "v_accvgpr_read_b32 v246, a26\n"
    "v_accvgpr_read_b32 v247, a27\n"
    "v_lshlrev_b32 v248, 16, v190\n"
    "v_lshlrev_b32 v249, 16, v106\n"
    "v_fmac_f32 v249, v248, v244\n"
    "v_and_b32 v248, 0xffff0000, v190\n"
    "v_and_b32 v250, 0xffff0000, v106\n"
    "v_fmac_f32 v250, v248, v245\n"
    "v_cvt_pk_bf16_f32 v106, v249, v250\n"
    "v_lshlrev_b32 v248, 16, v191\n"
    "v_lshlrev_b32 v249, 16, v107\n"
    "v_fmac_f32 v249, v248, v246\n"
    "v_and_b32 v248, 0xffff0000, v191\n"
    "v_and_b32 v250, 0xffff0000, v107\n"
    "v_fmac_f32 v250, v248, v247\n"
    "v_cvt_pk_bf16_f32 v107, v249, v250\n"
    "s_waitcnt lgkmcnt(10)\n"
    "v_accvgpr_read_b32 v244, a40\n"
    "v_accvgpr_read_b32 v245, a41\n"
    "v_accvgpr_read_b32 v246, a42\n"
    "v_accvgpr_read_b32 v247, a43\n"
    "v_lshlrev_b32 v248, 16, v192\n"
    "v_lshlrev_b32 v249, 16, v108\n"
    "v_fmac_f32 v249, v248, v244\n"
    "v_and_b32 v248, 0xffff0000, v192\n"
    "v_and_b32 v250, 0xffff0000, v108\n"
    "v_fmac_f32 v250, v248, v245\n"
    "v_cvt_pk_bf16_f32 v108, v249, v250\n"
    "v_lshlrev_b32 v248, 16, v193\n"
    "v_lshlrev_b32 v249, 16, v109\n"
    "v_fmac_f32 v249, v248, v246\n"
    "v_and_b32 v248, 0xffff0000, v193\n"
    "v_and_b32 v250, 0xffff0000, v109\n"
    "v_fmac_f32 v250, v248, v247\n"
    "v_cvt_pk_bf16_f32 v109, v249, v250\n"
    "s_waitcnt lgkmcnt(9)\n"
    "v_accvgpr_read_b32 v244, a56\n"
    "v_accvgpr_read_b32 v245, a57\n"
    "v_accvgpr_read_b32 v246, a58\n"
    "v_accvgpr_read_b32 v247, a59\n"
    "v_lshlrev_b32 v248, 16, v194\n"
    "v_lshlrev_b32 v249, 16, v110\n"
    "v_fmac_f32 v249, v248, v244\n"
    "v_and_b32 v248, 0xffff0000, v194\n"
    "v_and_b32 v250, 0xffff0000, v110\n"
    "v_fmac_f32 v250, v248, v245\n"
    "v_cvt_pk_bf16_f32 v110, v249, v250\n"
    "v_lshlrev_b32 v248, 16, v195\n"
    "v_lshlrev_b32 v249, 16, v111\n"
    "v_fmac_f32 v249, v248, v246\n"
    "v_and_b32 v248, 0xffff0000, v195\n"
    "v_and_b32 v250, 0xffff0000, v111\n"
    "v_fmac_f32 v250, v248, v247\n"
    "v_cvt_pk_bf16_f32 v111, v249, v250\n"
    "s_waitcnt lgkmcnt(8)\n"
    "v_accvgpr_read_b32 v244, a72\n"
    "v_accvgpr_read_b32 v245, a73\n"
    "v_accvgpr_read_b32 v246, a74\n"
    "v_accvgpr_read_b32 v247, a75\n"
    "v_lshlrev_b32 v248, 16, v196\n"
    "v_lshlrev_b32 v249, 16, v112\n"
    "v_fmac_f32 v249, v248, v244\n"
    "v_and_b32 v248, 0xffff0000, v196\n"
    "v_and_b32 v250, 0xffff0000, v112\n"
    "v_fmac_f32 v250, v248, v245\n"
    "v_cvt_pk_bf16_f32 v112, v249, v250\n"
    "v_lshlrev_b32 v248, 16, v197\n"
    "v_lshlrev_b32 v249, 16, v113\n"
    "v_fmac_f32 v249, v248, v246\n"
    "v_and_b32 v248, 0xffff0000, v197\n"
    "v_and_b32 v250, 0xffff0000, v113\n"
    "v_fmac_f32 v250, v248, v247\n"
    "v_cvt_pk_bf16_f32 v113, v249, v250\n"
    "s_waitcnt lgkmcnt(7)\n"
    "v_accvgpr_read_b32 v244, a88\n"
    "v_accvgpr_read_b32 v245, a89\n"
    "v_accvgpr_read_b32 v246, a90\n"
    "v_accvgpr_read_b32 v247, a91\n"
    "v_lshlrev_b32 v248, 16, v198\n"
    "v_lshlrev_b32 v249, 16, v114\n"
    "v_fmac_f32 v249, v248, v244\n"
    "v_and_b32 v248, 0xffff0000, v198\n"
    "v_and_b32 v250, 0xffff0000, v114\n"
    "v_fmac_f32 v250, v248, v245\n"
    "v_cvt_pk_bf16_f32 v114, v249, v250\n"
    "v_lshlrev_b32 v248, 16, v199\n"
    "v_lshlrev_b32 v249, 16, v115\n"
    "v_fmac_f32 v249, v248, v246\n"
    "v_and_b32 v248, 0xffff0000, v199\n"
    "v_and_b32 v250, 0xffff0000, v115\n"
    "v_fmac_f32 v250, v248, v247\n"
    "v_cvt_pk_bf16_f32 v115, v249, v250\n"
    "s_waitcnt lgkmcnt(6)\n"
    "v_accvgpr_read_b32 v244, a104\n"
    "v_accvgpr_read_b32 v245, a105\n"
    "v_accvgpr_read_b32 v246, a106\n"
    "v_accvgpr_read_b32 v247, a107\n"
    "v_lshlrev_b32 v248, 16, v200\n"
    "v_lshlrev_b32 v249, 16, v116\n"
    "v_fmac_f32 v249, v248, v244\n"
    "v_and_b32 v248, 0xffff0000, v200\n"
    "v_and_b32 v250, 0xffff0000, v116\n"
    "v_fmac_f32 v250, v248, v245\n"
    "v_cvt_pk_bf16_f32 v116, v249, v250\n"
    "v_lshlrev_b32 v248, 16, v201\n"
    "v_lshlrev_b32 v249, 16, v117\n"
    "v_fmac_f32 v249, v248, v246\n"
    "v_and_b32 v248, 0xffff0000, v201\n"
    "v_and_b32 v250, 0xffff0000, v117\n"
    "v_fmac_f32 v250, v248, v247\n"
    "v_cvt_pk_bf16_f32 v117, v249, v250\n"
    "s_waitcnt lgkmcnt(5)\n"
    "v_accvgpr_read_b32 v244, a120\n"
    "v_accvgpr_read_b32 v245, a121\n"
    "v_accvgpr_read_b32 v246, a122\n"
    "v_accvgpr_read_b32 v247, a123\n"
    "v_lshlrev_b32 v248, 16, v202\n"
    "v_lshlrev_b32 v249, 16, v118\n"
    "v_fmac_f32 v249, v248, v244\n"
    "v_and_b32 v248, 0xffff0000, v202\n"
    "v_and_b32 v250, 0xffff0000, v118\n"
    "v_fmac_f32 v250, v248, v245\n"
    "v_cvt_pk_bf16_f32 v118, v249, v250\n"
    "v_lshlrev_b32 v248, 16, v203\n"
    "v_lshlrev_b32 v249, 16, v119\n"
    "v_fmac_f32 v249, v248, v246\n"
    "v_and_b32 v248, 0xffff0000, v203\n"
    "v_and_b32 v250, 0xffff0000, v119\n"
    "v_fmac_f32 v250, v248, v247\n"
    "v_cvt_pk_bf16_f32 v119, v249, v250\n"
    "s_waitcnt lgkmcnt(4)\n"
    "v_accvgpr_read_b32 v244, a136\n"
    "v_accvgpr_read_b32 v245, a137\n"
    "v_accvgpr_read_b32 v246, a138\n"
    "v_accvgpr_read_b32 v247, a139\n"
    "v_lshlrev_b32 v248, 16, v204\n"
    "v_lshlrev_b32 v249, 16, v120\n"
    "v_fmac_f32 v249, v248, v244\n"
    "v_and_b32 v248, 0xffff0000, v204\n"
    "v_and_b32 v250, 0xffff0000, v120\n"
    "v_fmac_f32 v250, v248, v245\n"
    "v_cvt_pk_bf16_f32 v120, v249, v250\n"
    "v_lshlrev_b32 v248, 16, v205\n"
    "v_lshlrev_b32 v249, 16, v121\n"
    "v_fmac_f32 v249, v248, v246\n"
    "v_and_b32 v248, 0xffff0000, v205\n"
    "v_and_b32 v250, 0xffff0000, v121\n"
    "v_fmac_f32 v250, v248, v247\n"
    "v_cvt_pk_bf16_f32 v121, v249, v250\n"
    "s_waitcnt lgkmcnt(3)\n"
    "v_accvgpr_read_b32 v244, a152\n"
    "v_accvgpr_read_b32 v245, a153\n"
    "v_accvgpr_read_b32 v246, a154\n"
    "v_accvgpr_read_b32 v247, a155\n"
    "v_lshlrev_b32 v248, 16, v206\n"
    "v_lshlrev_b32 v249, 16, v122\n"
    "v_fmac_f32 v249, v248, v244\n"
    "v_and_b32 v248, 0xffff0000, v206\n"
    "v_and_b32 v250, 0xffff0000, v122\n"
    "v_fmac_f32 v250, v248, v245\n"
    "v_cvt_pk_bf16_f32 v122, v249, v250\n"
    "v_lshlrev_b32 v248, 16, v207\n"
    "v_lshlrev_b32 v249, 16, v123\n"
    "v_fmac_f32 v249, v248, v246\n"
    "v_and_b32 v248, 0xffff0000, v207\n"
    "v_and_b32 v250, 0xffff0000, v123\n"
    "v_fmac_f32 v250, v248, v247\n"
    "v_cvt_pk_bf16_f32 v123, v249, v250\n"
    "s_waitcnt lgkmcnt(2)\n"
    "v_accvgpr_read_b32 v244, a168\n"
    "v_accvgpr_read_b32 v245, a169\n"
    "v_accvgpr_read_b32 v246, a170\n"
    "v_accvgpr_read_b32 v247, a171\n"
    "v_lshlrev_b32 v248, 16, v208\n"
    "v_lshlrev_b32 v249, 16, v124\n"
    "v_fmac_f32 v249, v248, v244\n"
    "v_and_b32 v248, 0xffff0000, v208\n"
    "v_and_b32 v250, 0xffff0000, v124\n"
    "v_fmac_f32 v250, v248, v245\n"
    "v_cvt_pk_bf16_f32 v124, v249, v250\n"
    "v_lshlrev_b32 v248, 16, v209\n"
    "v_lshlrev_b32 v249, 16, v125\n"
    "v_fmac_f32 v249, v248, v246\n"
    "v_and_b32 v248, 0xffff0000, v209\n"
    "v_and_b32 v250, 0xffff0000, v125\n"
    "v_fmac_f32 v250, v248, v247\n"
    "v_cvt_pk_bf16_f32 v125, v249, v250\n"
    "s_waitcnt lgkmcnt(1)\n"
    "v_accvgpr_read_b32 v244, a184\n"
    "v_accvgpr_read_b32 v245, a185\n"
    "v_accvgpr_read_b32 v246, a186\n"
    "v_accvgpr_read_b32 v247, a187\n"
    "v_lshlrev_b32 v248, 16, v210\n"
    "v_lshlrev_b32 v249, 16, v126\n"
    "v_fmac_f32 v249, v248, v244\n"
    "v_and_b32 v248, 0xffff0000, v210\n"
    "v_and_b32 v250, 0xffff0000, v126\n"
    "v_fmac_f32 v250, v248, v245\n"
    "v_cvt_pk_bf16_f32 v126, v249, v250\n"
    "v_lshlrev_b32 v248, 16, v211\n"
    "v_lshlrev_b32 v249, 16, v127\n"
    "v_fmac_f32 v249, v248, v246\n"
    "v_and_b32 v248, 0xffff0000, v211\n"
    "v_and_b32 v250, 0xffff0000, v127\n"
    "v_fmac_f32 v250, v248, v247\n"
    "v_cvt_pk_bf16_f32 v127, v249, v250\n"
    "s_waitcnt lgkmcnt(0)\n"
    "v_accvgpr_read_b32 v244, a200\n"
    "v_accvgpr_read_b32 v245, a201\n"
    "v_accvgpr_read_b32 v246, a202\n"
    "v_accvgpr_read_b32 v247, a203\n"
    "v_lshlrev_b32 v248, 16, v212\n"
    "v_lshlrev_b32 v249, 16, v128\n"
    "v_fmac_f32 v249, v248, v244\n"
    "v_and_b32 v248, 0xffff0000, v212\n"
    "v_and_b32 v250, 0xffff0000, v128\n"
    "v_fmac_f32 v250, v248, v245\n"
    "v_cvt_pk_bf16_f32 v128, v249, v250\n"
    "v_lshlrev_b32 v248, 16, v213\n"
    "v_lshlrev_b32 v249, 16, v129\n"
    "v_fmac_f32 v249, v248, v246\n"
    "v_and_b32 v248, 0xffff0000, v213\n"
    "v_and_b32 v250, 0xffff0000, v129\n"
    "v_fmac_f32 v250, v248, v247\n"
    "v_cvt_pk_bf16_f32 v129, v249, v250\n"
    "ds_write_b64 v255, v[104:105] offset:0\n"
    "ds_write_b64 v255, v[106:107] offset:32\n"
    "ds_write_b64 v255, v[108:109] offset:64\n"
    "ds_write_b64 v255, v[110:111] offset:96\n"
    "ds_write_b64 v255, v[112:113] offset:128\n"
    "ds_write_b64 v255, v[114:115] offset:160\n"
    "ds_write_b64 v255, v[116:117] offset:192\n"
    "ds_write_b64 v255, v[118:119] offset:224\n"
    "ds_write_b64 v255, v[120:121] offset:256\n"
    "ds_write_b64 v255, v[122:123] offset:288\n"
    "ds_write_b64 v255, v[124:125] offset:320\n"
    "ds_write_b64 v255, v[126:127] offset:352\n"
    "ds_write_b64 v255, v[128:129] offset:384\n"
    "s_waitcnt lgkmcnt(0)\n"
    "ds_read_b64 v[104:105], v255 offset:26880\n"
    "ds_read_b64 v[106:107], v255 offset:26912\n"
    "ds_read_b64 v[108:109], v255 offset:26944\n"
    "ds_read_b64 v[110:111], v255 offset:26976\n"
    "ds_read_b64 v[112:113], v255 offset:27008\n"
    "ds_read_b64 v[114:115], v255 offset:27040\n"
    "ds_read_b64 v[116:117], v255 offset:27072\n"
    "ds_read_b64 v[118:119], v255 offset:27104\n"
    "ds_read_b64 v[120:121], v255 offset:27136\n"
    "ds_read_b64 v[122:123], v255 offset:27168\n"
    "ds_read_b64 v[124:125], v255 offset:27200\n"
    "ds_read_b64 v[126:127], v255 offset:27232\n"
    "ds_read_b64 v[128:129], v255 offset:27264\n"
    "s_waitcnt lgkmcnt(12)\n"
    "v_accvgpr_read_b32 v244, a12\n"
    "v_accvgpr_read_b32 v245, a13\n"
    "v_accvgpr_read_b32 v246, a14\n"
    "v_accvgpr_read_b32 v247, a15\n"
    "v_lshlrev_b32 v248, 16, v214\n"
    "v_lshlrev_b32 v249, 16, v104\n"
    "v_fmac_f32 v249, v248, v244\n"
    "v_and_b32 v248, 0xffff0000, v214\n"
    "v_and_b32 v250, 0xffff0000, v104\n"
    "v_fmac_f32 v250, v248, v245\n"
    "v_cvt_pk_bf16_f32 v104, v249, v250\n"
    "v_lshlrev_b32 v248, 16, v215\n"
    "v_lshlrev_b32 v249, 16, v105\n"
    "v_fmac_f32 v249, v248, v246\n"
    "v_and_b32 v248, 0xffff0000, v215\n"
    "v_and_b32 v250, 0xffff0000, v105\n"
    "v_fmac_f32 v250, v248, v247\n"
    "v_cvt_pk_bf16_f32 v105, v249, v250\n"
    "s_waitcnt lgkmcnt(11)\n"
    "v_accvgpr_read_b32 v244, a28\n"
    "v_accvgpr_read_b32 v245, a29\n"
    "v_accvgpr_read_b32 v246, a30\n"
    "v_accvgpr_read_b32 v247, a31\n"
    "v_lshlrev_b32 v248, 16, v216\n"
    "v_lshlrev_b32 v249, 16, v106\n"
    "v_fmac_f32 v249, v248, v244\n"
    "v_and_b32 v248, 0xffff0000, v216\n"
    "v_and_b32 v250, 0xffff0000, v106\n"
    "v_fmac_f32 v250, v248, v245\n"
    "v_cvt_pk_bf16_f32 v106, v249, v250\n"
    "v_lshlrev_b32 v248, 16, v217\n"
    "v_lshlrev_b32 v249, 16, v107\n"
    "v_fmac_f32 v249, v248, v246\n"
    "v_and_b32 v248, 0xffff0000, v217\n"
    "v_and_b32 v250, 0xffff0000, v107\n"
    "v_fmac_f32 v250, v248, v247\n"
    "v_cvt_pk_bf16_f32 v107, v249, v250\n"
    "s_waitcnt lgkmcnt(10)\n"
    "v_accvgpr_read_b32 v244, a44\n"
    "v_accvgpr_read_b32 v245, a45\n"
    "v_accvgpr_read_b32 v246, a46\n"
    "v_accvgpr_read_b32 v247, a47\n"
    "v_lshlrev_b32 v248, 16, v218\n"
    "v_lshlrev_b32 v249, 16, v108\n"
    "v_fmac_f32 v249, v248, v244\n"
    "v_and_b32 v248, 0xffff0000, v218\n"
    "v_and_b32 v250, 0xffff0000, v108\n"
    "v_fmac_f32 v250, v248, v245\n"
    "v_cvt_pk_bf16_f32 v108, v249, v250\n"
    "v_lshlrev_b32 v248, 16, v219\n"
    "v_lshlrev_b32 v249, 16, v109\n"
    "v_fmac_f32 v249, v248, v246\n"
    "v_and_b32 v248, 0xffff0000, v219\n"
    "v_and_b32 v250, 0xffff0000, v109\n"
    "v_fmac_f32 v250, v248, v247\n"
    "v_cvt_pk_bf16_f32 v109, v249, v250\n"
    "s_waitcnt lgkmcnt(9)\n"
    "v_accvgpr_read_b32 v244, a60\n"
    "v_accvgpr_read_b32 v245, a61\n"
    "v_accvgpr_read_b32 v246, a62\n"
    "v_accvgpr_read_b32 v247, a63\n"
    "v_lshlrev_b32 v248, 16, v220\n"
    "v_lshlrev_b32 v249, 16, v110\n"
    "v_fmac_f32 v249, v248, v244\n"
    "v_and_b32 v248, 0xffff0000, v220\n"
    "v_and_b32 v250, 0xffff0000, v110\n"
    "v_fmac_f32 v250, v248, v245\n"
    "v_cvt_pk_bf16_f32 v110, v249, v250\n"
    "v_lshlrev_b32 v248, 16, v221\n"
    "v_lshlrev_b32 v249, 16, v111\n"
    "v_fmac_f32 v249, v248, v246\n"
    "v_and_b32 v248, 0xffff0000, v221\n"
    "v_and_b32 v250, 0xffff0000, v111\n"
    "v_fmac_f32 v250, v248, v247\n"
    "v_cvt_pk_bf16_f32 v111, v249, v250\n"
    "s_waitcnt lgkmcnt(8)\n"
    "v_accvgpr_read_b32 v244, a76\n"
    "v_accvgpr_read_b32 v245, a77\n"
    "v_accvgpr_read_b32 v246, a78\n"
    "v_accvgpr_read_b32 v247, a79\n"
    "v_lshlrev_b32 v248, 16, v222\n"
    "v_lshlrev_b32 v249, 16, v112\n"
    "v_fmac_f32 v249, v248, v244\n"
    "v_and_b32 v248, 0xffff0000, v222\n"
    "v_and_b32 v250, 0xffff0000, v112\n"
    "v_fmac_f32 v250, v248, v245\n"
    "v_cvt_pk_bf16_f32 v112, v249, v250\n"
    "v_lshlrev_b32 v248, 16, v223\n"
    "v_lshlrev_b32 v249, 16, v113\n"
    "v_fmac_f32 v249, v248, v246\n"
    "v_and_b32 v248, 0xffff0000, v223\n"
    "v_and_b32 v250, 0xffff0000, v113\n"
    "v_fmac_f32 v250, v248, v247\n"
    "v_cvt_pk_bf16_f32 v113, v249, v250\n"
    "s_waitcnt lgkmcnt(7)\n"
    "v_accvgpr_read_b32 v244, a92\n"
    "v_accvgpr_read_b32 v245, a93\n"
    "v_accvgpr_read_b32 v246, a94\n"
    "v_accvgpr_read_b32 v247, a95\n"
    "v_lshlrev_b32 v248, 16, v224\n"
    "v_lshlrev_b32 v249, 16, v114\n"
    "v_fmac_f32 v249, v248, v244\n"
    "v_and_b32 v248, 0xffff0000, v224\n"
    "v_and_b32 v250, 0xffff0000, v114\n"
    "v_fmac_f32 v250, v248, v245\n"
    "v_cvt_pk_bf16_f32 v114, v249, v250\n"
    "v_lshlrev_b32 v248, 16, v225\n"
    "v_lshlrev_b32 v249, 16, v115\n"
    "v_fmac_f32 v249, v248, v246\n"
    "v_and_b32 v248, 0xffff0000, v225\n"
    "v_and_b32 v250, 0xffff0000, v115\n"
    "v_fmac_f32 v250, v248, v247\n"
    "v_cvt_pk_bf16_f32 v115, v249, v250\n"
    "s_waitcnt lgkmcnt(6)\n"
    "v_accvgpr_read_b32 v244, a108\n"
    "v_accvgpr_read_b32 v245, a109\n"
    "v_accvgpr_read_b32 v246, a110\n"
    "v_accvgpr_read_b32 v247, a111\n"
    "v_lshlrev_b32 v248, 16, v226\n"
    "v_lshlrev_b32 v249, 16, v116\n"
    "v_fmac_f32 v249, v248, v244\n"
    "v_and_b32 v248, 0xffff0000, v226\n"
    "v_and_b32 v250, 0xffff0000, v116\n"
    "v_fmac_f32 v250, v248, v245\n"
    "v_cvt_pk_bf16_f32 v116, v249, v250\n"
    "v_lshlrev_b32 v248, 16, v227\n"
    "v_lshlrev_b32 v249, 16, v117\n"
    "v_fmac_f32 v249, v248, v246\n"
    "v_and_b32 v248, 0xffff0000, v227\n"
    "v_and_b32 v250, 0xffff0000, v117\n"
    "v_fmac_f32 v250, v248, v247\n"
    "v_cvt_pk_bf16_f32 v117, v249, v250\n"
    "s_waitcnt lgkmcnt(5)\n"
    "v_accvgpr_read_b32 v244, a124\n"
    "v_accvgpr_read_b32 v245, a125\n"
    "v_accvgpr_read_b32 v246, a126\n"
    "v_accvgpr_read_b32 v247, a127\n"
    "v_lshlrev_b32 v248, 16, v228\n"
    "v_lshlrev_b32 v249, 16, v118\n"
    "v_fmac_f32 v249, v248, v244\n"
    "v_and_b32 v248, 0xffff0000, v228\n"
    "v_and_b32 v250, 0xffff0000, v118\n"
    "v_fmac_f32 v250, v248, v245\n"
    "v_cvt_pk_bf16_f32 v118, v249, v250\n"
    "v_lshlrev_b32 v248, 16, v229\n"
    "v_lshlrev_b32 v249, 16, v119\n"
    "v_fmac_f32 v249, v248, v246\n"
    "v_and_b32 v248, 0xffff0000, v229\n"
    "v_and_b32 v250, 0xffff0000, v119\n"
    "v_fmac_f32 v250, v248, v247\n"
    "v_cvt_pk_bf16_f32 v119, v249, v250\n"
    "s_waitcnt lgkmcnt(4)\n"
    "v_accvgpr_read_b32 v244, a140\n"
    "v_accvgpr_read_b32 v245, a141\n"
    "v_accvgpr_read_b32 v246, a142\n"
    "v_accvgpr_read_b32 v247, a143\n"
    "v_lshlrev_b32 v248, 16, v230\n"
    "v_lshlrev_b32 v249, 16, v120\n"
    "v_fmac_f32 v249, v248, v244\n"
    "v_and_b32 v248, 0xffff0000, v230\n"
    "v_and_b32 v250, 0xffff0000, v120\n"
    "v_fmac_f32 v250, v248, v245\n"
    "v_cvt_pk_bf16_f32 v120, v249, v250\n"
    "v_lshlrev_b32 v248, 16, v231\n"
    "v_lshlrev_b32 v249, 16, v121\n"
    "v_fmac_f32 v249, v248, v246\n"
    "v_and_b32 v248, 0xffff0000, v231\n"
    "v_and_b32 v250, 0xffff0000, v121\n"
    "v_fmac_f32 v250, v248, v247\n"
    "v_cvt_pk_bf16_f32 v121, v249, v250\n"
    "s_waitcnt lgkmcnt(3)\n"
    "v_accvgpr_read_b32 v244, a156\n"
    "v_accvgpr_read_b32 v245, a157\n"
    "v_accvgpr_read_b32 v246, a158\n"
    "v_accvgpr_read_b32 v247, a159\n"
    "v_lshlrev_b32 v248, 16, v232\n"
    "v_lshlrev_b32 v249, 16, v122\n"
    "v_fmac_f32 v249, v248, v244\n"
    "v_and_b32 v248, 0xffff0000, v232\n"
    "v_and_b32 v250, 0xffff0000, v122\n"
    "v_fmac_f32 v250, v248, v245\n"
    "v_cvt_pk_bf16_f32 v122, v249, v250\n"
    "v_lshlrev_b32 v248, 16, v233\n"
    "v_lshlrev_b32 v249, 16, v123\n"
    "v_fmac_f32 v249, v248, v246\n"
    "v_and_b32 v248, 0xffff0000, v233\n"
    "v_and_b32 v250, 0xffff0000, v123\n"
    "v_fmac_f32 v250, v248, v247\n"
    "v_cvt_pk_bf16_f32 v123, v249, v250\n"
    "s_waitcnt lgkmcnt(2)\n"
    "v_accvgpr_read_b32 v244, a172\n"
    "v_accvgpr_read_b32 v245, a173\n"
    "v_accvgpr_read_b32 v246, a174\n"
    "v_accvgpr_read_b32 v247, a175\n"
    "v_lshlrev_b32 v248, 16, v234\n"
    "v_lshlrev_b32 v249, 16, v124\n"
    "v_fmac_f32 v249, v248, v244\n"
    "v_and_b32 v248, 0xffff0000, v234\n"
    "v_and_b32 v250, 0xffff0000, v124\n"
    "v_fmac_f32 v250, v248, v245\n"
    "v_cvt_pk_bf16_f32 v124, v249, v250\n"
    "v_lshlrev_b32 v248, 16, v235\n"
    "v_lshlrev_b32 v249, 16, v125\n"
    "v_fmac_f32 v249, v248, v246\n"
    "v_and_b32 v248, 0xffff0000, v235\n"
    "v_and_b32 v250, 0xffff0000, v125\n"
    "v_fmac_f32 v250, v248, v247\n"
    "v_cvt_pk_bf16_f32 v125, v249, v250\n"
    "s_waitcnt lgkmcnt(1)\n"
    "v_accvgpr_read_b32 v244, a188\n"
    "v_accvgpr_read_b32 v245, a189\n"
    "v_accvgpr_read_b32 v246, a190\n"
    "v_accvgpr_read_b32 v247, a191\n"
    "v_lshlrev_b32 v248, 16, v236\n"
    "v_lshlrev_b32 v249, 16, v126\n"
    "v_fmac_f32 v249, v248, v244\n"
    "v_and_b32 v248, 0xffff0000, v236\n"
    "v_and_b32 v250, 0xffff0000, v126\n"
    "v_fmac_f32 v250, v248, v245\n"
    "v_cvt_pk_bf16_f32 v126, v249, v250\n"
    "v_lshlrev_b32 v248, 16, v237\n"
    "v_lshlrev_b32 v249, 16, v127\n"
    "v_fmac_f32 v249, v248, v246\n"
    "v_and_b32 v248, 0xffff0000, v237\n"
    "v_and_b32 v250, 0xffff0000, v127\n"
    "v_fmac_f32 v250, v248, v247\n"
    "v_cvt_pk_bf16_f32 v127, v249, v250\n"
    "s_waitcnt lgkmcnt(0)\n"
    "v_accvgpr_read_b32 v244, a204\n"
    "v_accvgpr_read_b32 v245, a205\n"
    "v_accvgpr_read_b32 v246, a206\n"
    "v_accvgpr_read_b32 v247, a207\n"
    "v_lshlrev_b32 v248, 16, v238\n"
    "v_lshlrev_b32 v249, 16, v128\n"
    "v_fmac_f32 v249, v248, v244\n"
    "v_and_b32 v248, 0xffff0000, v238\n"
    "v_and_b32 v250, 0xffff0000, v128\n"
    "v_fmac_f32 v250, v248, v245\n"
    "v_cvt_pk_bf16_f32 v128, v249, v250\n"
    "v_lshlrev_b32 v248, 16, v239\n"
    "v_lshlrev_b32 v249, 16, v129\n"
    "v_fmac_f32 v249, v248, v246\n"
    "v_and_b32 v248, 0xffff0000, v239\n"
    "v_and_b32 v250, 0xffff0000, v129\n"
    "v_fmac_f32 v250, v248, v247\n"
    "v_cvt_pk_bf16_f32 v129, v249, v250\n"
    "ds_write_b64 v255, v[104:105] offset:26880\n"
    "ds_write_b64 v255, v[106:107] offset:26912\n"
    "ds_write_b64 v255, v[108:109] offset:26944\n"
    "ds_write_b64 v255, v[110:111] offset:26976\n"
    "ds_write_b64 v255, v[112:113] offset:27008\n"
    "ds_write_b64 v255, v[114:115] offset:27040\n"
    "ds_write_b64 v255, v[116:117] offset:27072\n"
    "ds_write_b64 v255, v[118:119] offset:27104\n"
    "ds_write_b64 v255, v[120:121] offset:27136\n"
    "ds_write_b64 v255, v[122:123] offset:27168\n"
    "ds_write_b64 v255, v[124:125] offset:27200\n"
    "ds_write_b64 v255, v[126:127] offset:27232\n"
    "ds_write_b64 v255, v[128:129] offset:27264\n"
    "s_waitcnt lgkmcnt(0)\n"
    "v_add_u32 v253, 3328, v253\n"
    "global_load_dwordx2 v[104:105], v253, s[52:53] offset:0\n"
    "global_load_dwordx2 v[106:107], v253, s[52:53] offset:32\n"
    "global_load_dwordx2 v[108:109], v253, s[52:53] offset:64\n"
    "global_load_dwordx2 v[110:111], v253, s[52:53] offset:96\n"
    "global_load_dwordx2 v[112:113], v253, s[52:53] offset:128\n"
    "global_load_dwordx2 v[114:115], v253, s[52:53] offset:160\n"
    "global_load_dwordx2 v[116:117], v253, s[52:53] offset:192\n"
    "global_load_dwordx2 v[118:119], v253, s[52:53] offset:224\n"
    "global_load_dwordx2 v[120:121], v253, s[52:53] offset:256\n"
    "global_load_dwordx2 v[122:123], v253, s[52:53] offset:288\n"
    "global_load_dwordx2 v[124:125], v253, s[52:53] offset:320\n"
    "global_load_dwordx2 v[126:127], v253, s[52:53] offset:352\n"
    "global_load_dwordx2 v[128:129], v253, s[52:53] offset:384\n"
    "s_barrier\n"
    "; ===================== layer 2 =====================\n"
    "s_waitcnt vmcnt(0)\n"
    "v_lshlrev_b32 v244, 16, v104\n"
    "v_and_b32 v245, 0xffff0000, v104\n"
    "v_lshlrev_b32 v246, 16, v105\n"
    "v_and_b32 v247, 0xffff0000, v105\n"
    "v_accvgpr_write_b32 a0, v244\n"
    "v_accvgpr_write_b32 a1, v245\n"
    "v_accvgpr_write_b32 a2, v246\n"
    "v_accvgpr_write_b32 a3, v247\n"
    "v_accvgpr_write_b32 a4, v244\n"
    "v_accvgpr_write_b32 a5, v245\n"
    "v_accvgpr_write_b32 a6, v246\n"
    "v_accvgpr_write_b32 a7, v247\n"
    "v_accvgpr_write_b32 a8, v244\n"
    "v_accvgpr_write_b32 a9, v245\n"
    "v_accvgpr_write_b32 a10, v246\n"
    "v_accvgpr_write_b32 a11, v247\n"
    "v_accvgpr_write_b32 a12, v244\n"
    "v_accvgpr_write_b32 a13, v245\n"
    "v_accvgpr_write_b32 a14, v246\n"
    "v_accvgpr_write_b32 a15, v247\n"
    "v_lshlrev_b32 v244, 16, v106\n"
    "v_and_b32 v245, 0xffff0000, v106\n"
    "v_lshlrev_b32 v246, 16, v107\n"
    "v_and_b32 v247, 0xffff0000, v107\n"
    "v_accvgpr_write_b32 a16, v244\n"
    "v_accvgpr_write_b32 a17, v245\n"
    "v_accvgpr_write_b32 a18, v246\n"
    "v_accvgpr_write_b32 a19, v247\n"
    "v_accvgpr_write_b32 a20, v244\n"
    "v_accvgpr_write_b32 a21, v245\n"
    "v_accvgpr_write_b32 a22, v246\n"
    "v_accvgpr_write_b32 a23, v247\n"
    "v_accvgpr_write_b32 a24, v244\n"
    "v_accvgpr_write_b32 a25, v245\n"
    "v_accvgpr_write_b32 a26, v246\n"
    "v_accvgpr_write_b32 a27, v247\n"
    "v_accvgpr_write_b32 a28, v244\n"
    "v_accvgpr_write_b32 a29, v245\n"
    "v_accvgpr_write_b32 a30, v246\n"
    "v_accvgpr_write_b32 a31, v247\n"
    "v_lshlrev_b32 v244, 16, v108\n"
    "v_and_b32 v245, 0xffff0000, v108\n"
    "v_lshlrev_b32 v246, 16, v109\n"
    "v_and_b32 v247, 0xffff0000, v109\n"
    "v_accvgpr_write_b32 a32, v244\n"
    "v_accvgpr_write_b32 a33, v245\n"
    "v_accvgpr_write_b32 a34, v246\n"
    "v_accvgpr_write_b32 a35, v247\n"
    "v_accvgpr_write_b32 a36, v244\n"
    "v_accvgpr_write_b32 a37, v245\n"
    "v_accvgpr_write_b32 a38, v246\n"
    "v_accvgpr_write_b32 a39, v247\n"
    "v_accvgpr_write_b32 a40, v244\n"
    "v_accvgpr_write_b32 a41, v245\n"
    "v_accvgpr_write_b32 a42, v246\n"
    "v_accvgpr_write_b32 a43, v247\n"
    "v_accvgpr_write_b32 a44, v244\n"
    "v_accvgpr_write_b32 a45, v245\n"
    "v_accvgpr_write_b32 a46, v246\n"
    "v_accvgpr_write_b32 a47, v247\n"
    "v_lshlrev_b32 v244, 16, v110\n"
    "v_and_b32 v245, 0xffff0000, v110\n"
    "v_lshlrev_b32 v246, 16, v111\n"
    "v_and_b32 v247, 0xffff0000, v111\n"
    "v_accvgpr_write_b32 a48, v244\n"
    "v_accvgpr_write_b32 a49, v245\n"
    "v_accvgpr_write_b32 a50, v246\n"
    "v_accvgpr_write_b32 a51, v247\n"
    "v_accvgpr_write_b32 a52, v244\n"
    "v_accvgpr_write_b32 a53, v245\n"
    "v_accvgpr_write_b32 a54, v246\n"
    "v_accvgpr_write_b32 a55, v247\n"
    "v_accvgpr_write_b32 a56, v244\n"
    "v_accvgpr_write_b32 a57, v245\n"
    "v_accvgpr_write_b32 a58, v246\n"
    "v_accvgpr_write_b32 a59, v247\n"
    "v_accvgpr_write_b32 a60, v244\n"
    "v_accvgpr_write_b32 a61, v245\n"
    "v_accvgpr_write_b32 a62, v246\n"
    "v_accvgpr_write_b32 a63, v247\n"
    "v_lshlrev_b32 v244, 16, v112\n"
    "v_and_b32 v245, 0xffff0000, v112\n"
    "v_lshlrev_b32 v246, 16, v113\n"
    "v_and_b32 v247, 0xffff0000, v113\n"
    "v_accvgpr_write_b32 a64, v244\n"
    "v_accvgpr_write_b32 a65, v245\n"
    "v_accvgpr_write_b32 a66, v246\n"
    "v_accvgpr_write_b32 a67, v247\n"
    "v_accvgpr_write_b32 a68, v244\n"
    "v_accvgpr_write_b32 a69, v245\n"
    "v_accvgpr_write_b32 a70, v246\n"
    "v_accvgpr_write_b32 a71, v247\n"
    "v_accvgpr_write_b32 a72, v244\n"
    "v_accvgpr_write_b32 a73, v245\n"
    "v_accvgpr_write_b32 a74, v246\n"
    "v_accvgpr_write_b32 a75, v247\n"
    "v_accvgpr_write_b32 a76, v244\n"
    "v_accvgpr_write_b32 a77, v245\n"
    "v_accvgpr_write_b32 a78, v246\n"
    "v_accvgpr_write_b32 a79, v247\n"
    "v_lshlrev_b32 v244, 16, v114\n"
    "v_and_b32 v245, 0xffff0000, v114\n"
    "v_lshlrev_b32 v246, 16, v115\n"
    "v_and_b32 v247, 0xffff0000, v115\n"
    "v_accvgpr_write_b32 a80, v244\n"
    "v_accvgpr_write_b32 a81, v245\n"
    "v_accvgpr_write_b32 a82, v246\n"
    "v_accvgpr_write_b32 a83, v247\n"
    "v_accvgpr_write_b32 a84, v244\n"
    "v_accvgpr_write_b32 a85, v245\n"
    "v_accvgpr_write_b32 a86, v246\n"
    "v_accvgpr_write_b32 a87, v247\n"
    "v_accvgpr_write_b32 a88, v244\n"
    "v_accvgpr_write_b32 a89, v245\n"
    "v_accvgpr_write_b32 a90, v246\n"
    "v_accvgpr_write_b32 a91, v247\n"
    "v_accvgpr_write_b32 a92, v244\n"
    "v_accvgpr_write_b32 a93, v245\n"
    "v_accvgpr_write_b32 a94, v246\n"
    "v_accvgpr_write_b32 a95, v247\n"
    "v_lshlrev_b32 v244, 16, v116\n"
    "v_and_b32 v245, 0xffff0000, v116\n"
    "v_lshlrev_b32 v246, 16, v117\n"
    "v_and_b32 v247, 0xffff0000, v117\n"
    "v_accvgpr_write_b32 a96, v244\n"
    "v_accvgpr_write_b32 a97, v245\n"
    "v_accvgpr_write_b32 a98, v246\n"
    "v_accvgpr_write_b32 a99, v247\n"
    "v_accvgpr_write_b32 a100, v244\n"
    "v_accvgpr_write_b32 a101, v245\n"
    "v_accvgpr_write_b32 a102, v246\n"
    "v_accvgpr_write_b32 a103, v247\n"
    "v_accvgpr_write_b32 a104, v244\n"
    "v_accvgpr_write_b32 a105, v245\n"
    "v_accvgpr_write_b32 a106, v246\n"
    "v_accvgpr_write_b32 a107, v247\n"
    "v_accvgpr_write_b32 a108, v244\n"
    "v_accvgpr_write_b32 a109, v245\n"
    "v_accvgpr_write_b32 a110, v246\n"
    "v_accvgpr_write_b32 a111, v247\n"
    "v_lshlrev_b32 v244, 16, v118\n"
    "v_and_b32 v245, 0xffff0000, v118\n"
    "v_lshlrev_b32 v246, 16, v119\n"
    "v_and_b32 v247, 0xffff0000, v119\n"
    "v_accvgpr_write_b32 a112, v244\n"
    "v_accvgpr_write_b32 a113, v245\n"
    "v_accvgpr_write_b32 a114, v246\n"
    "v_accvgpr_write_b32 a115, v247\n"
    "v_accvgpr_write_b32 a116, v244\n"
    "v_accvgpr_write_b32 a117, v245\n"
    "v_accvgpr_write_b32 a118, v246\n"
    "v_accvgpr_write_b32 a119, v247\n"
    "v_accvgpr_write_b32 a120, v244\n"
    "v_accvgpr_write_b32 a121, v245\n"
    "v_accvgpr_write_b32 a122, v246\n"
    "v_accvgpr_write_b32 a123, v247\n"
    "v_accvgpr_write_b32 a124, v244\n"
    "v_accvgpr_write_b32 a125, v245\n"
    "v_accvgpr_write_b32 a126, v246\n"
    "v_accvgpr_write_b32 a127, v247\n"
    "v_lshlrev_b32 v244, 16, v120\n"
    "v_and_b32 v245, 0xffff0000, v120\n"
    "v_lshlrev_b32 v246, 16, v121\n"
    "v_and_b32 v247, 0xffff0000, v121\n"
    "v_accvgpr_write_b32 a128, v244\n"
    "v_accvgpr_write_b32 a129, v245\n"
    "v_accvgpr_write_b32 a130, v246\n"
    "v_accvgpr_write_b32 a131, v247\n"
    "v_accvgpr_write_b32 a132, v244\n"
    "v_accvgpr_write_b32 a133, v245\n"
    "v_accvgpr_write_b32 a134, v246\n"
    "v_accvgpr_write_b32 a135, v247\n"
    "v_accvgpr_write_b32 a136, v244\n"
    "v_accvgpr_write_b32 a137, v245\n"
    "v_accvgpr_write_b32 a138, v246\n"
    "v_accvgpr_write_b32 a139, v247\n"
    "v_accvgpr_write_b32 a140, v244\n"
    "v_accvgpr_write_b32 a141, v245\n"
    "v_accvgpr_write_b32 a142, v246\n"
    "v_accvgpr_write_b32 a143, v247\n"
    "v_lshlrev_b32 v244, 16, v122\n"
    "v_and_b32 v245, 0xffff0000, v122\n"
    "v_lshlrev_b32 v246, 16, v123\n"
    "v_and_b32 v247, 0xffff0000, v123\n"
    "v_accvgpr_write_b32 a144, v244\n"
    "v_accvgpr_write_b32 a145, v245\n"
    "v_accvgpr_write_b32 a146, v246\n"
    "v_accvgpr_write_b32 a147, v247\n"
    "v_accvgpr_write_b32 a148, v244\n"
    "v_accvgpr_write_b32 a149, v245\n"
    "v_accvgpr_write_b32 a150, v246\n"
    "v_accvgpr_write_b32 a151, v247\n"
    "v_accvgpr_write_b32 a152, v244\n"
    "v_accvgpr_write_b32 a153, v245\n"
    "v_accvgpr_write_b32 a154, v246\n"
    "v_accvgpr_write_b32 a155, v247\n"
    "v_accvgpr_write_b32 a156, v244\n"
    "v_accvgpr_write_b32 a157, v245\n"
    "v_accvgpr_write_b32 a158, v246\n"
    "v_accvgpr_write_b32 a159, v247\n"
    "v_lshlrev_b32 v244, 16, v124\n"
    "v_and_b32 v245, 0xffff0000, v124\n"
    "v_lshlrev_b32 v246, 16, v125\n"
    "v_and_b32 v247, 0xffff0000, v125\n"
    "v_accvgpr_write_b32 a160, v244\n"
    "v_accvgpr_write_b32 a161, v245\n"
    "v_accvgpr_write_b32 a162, v246\n"
    "v_accvgpr_write_b32 a163, v247\n"
    "v_accvgpr_write_b32 a164, v244\n"
    "v_accvgpr_write_b32 a165, v245\n"
    "v_accvgpr_write_b32 a166, v246\n"
    "v_accvgpr_write_b32 a167, v247\n"
    "v_accvgpr_write_b32 a168, v244\n"
    "v_accvgpr_write_b32 a169, v245\n"
    "v_accvgpr_write_b32 a170, v246\n"
    "v_accvgpr_write_b32 a171, v247\n"
    "v_accvgpr_write_b32 a172, v244\n"
    "v_accvgpr_write_b32 a173, v245\n"
    "v_accvgpr_write_b32 a174, v246\n"
    "v_accvgpr_write_b32 a175, v247\n"
    "v_lshlrev_b32 v244, 16, v126\n"
    "v_and_b32 v245, 0xffff0000, v126\n"
    "v_lshlrev_b32 v246, 16, v127\n"
    "v_and_b32 v247, 0xffff0000, v127\n"
    "v_accvgpr_write_b32 a176, v244\n"
    "v_accvgpr_write_b32 a177, v245\n"
    "v_accvgpr_write_b32 a178, v246\n"
    "v_accvgpr_write_b32 a179, v247\n"
    "v_accvgpr_write_b32 a180, v244\n"
    "v_accvgpr_write_b32 a181, v245\n"
    "v_accvgpr_write_b32 a182, v246\n"
    "v_accvgpr_write_b32 a183, v247\n"
    "v_accvgpr_write_b32 a184, v244\n"
    "v_accvgpr_write_b32 a185, v245\n"
    "v_accvgpr_write_b32 a186, v246\n"
    "v_accvgpr_write_b32 a187, v247\n"
    "v_accvgpr_write_b32 a188, v244\n"
    "v_accvgpr_write_b32 a189, v245\n"
    "v_accvgpr_write_b32 a190, v246\n"
    "v_accvgpr_write_b32 a191, v247\n"
    "v_lshlrev_b32 v244, 16, v128\n"
    "v_and_b32 v245, 0xffff0000, v128\n"
    "v_lshlrev_b32 v246, 16, v129\n"
    "v_and_b32 v247, 0xffff0000, v129\n"
    "v_accvgpr_write_b32 a192, v244\n"
    "v_accvgpr_write_b32 a193, v245\n"
    "v_accvgpr_write_b32 a194, v246\n"
    "v_accvgpr_write_b32 a195, v247\n"
    "v_accvgpr_write_b32 a196, v244\n"
    "v_accvgpr_write_b32 a197, v245\n"
    "v_accvgpr_write_b32 a198, v246\n"
    "v_accvgpr_write_b32 a199, v247\n"
    "v_accvgpr_write_b32 a200, v244\n"
    "v_accvgpr_write_b32 a201, v245\n"
    "v_accvgpr_write_b32 a202, v246\n"
    "v_accvgpr_write_b32 a203, v247\n"
    "v_accvgpr_write_b32 a204, v244\n"
    "v_accvgpr_write_b32 a205, v245\n"
    "v_accvgpr_write_b32 a206, v246\n"
    "v_accvgpr_write_b32 a207, v247\n"
    "v_and_b32 v246, 15, v240\n"
    "v_mul_u32_u24 v242, 1680, v246\n"
    "v_lshrrev_b32 v247, 4, v240\n"
    "v_lshlrev_b32 v247, 4, v247\n"
    "v_add_u32 v242, v242, v247\n"
    "v_add_u32 v243, 53760, v242\n"
    "ds_read_b128 v[104:107], v242 offset:0\n"
    "ds_read_b128 v[108:111], v242 offset:26880\n"
    "ds_read_b128 v[112:115], v243 offset:0\n"
    "ds_read_b128 v[116:119], v243 offset:26880\n"
    "s_mov_b32 s57, 13\n"
    "L_loop_2_%=:\n"
    "s_add_u32 s42, s40, 4096\n"
    "s_addc_u32 s43, s41, 0\n"
    "s_add_u32 s44, s40, 12288\n"
    "s_addc_u32 s45, s41, 0\n"
    "s_add_u32 s46, s40, 20480\n"
    "s_addc_u32 s47, s41, 0\n"
    "s_add_u32 s48, s40, 28672\n"
    "s_addc_u32 s49, s41, 0\n"
    "s_add_u32 s40, s40, 26624\n"
    "s_addc_u32 s41, s41, 0\n"
    "ds_read_b128 v[120:123], v242 offset:64\n"
    "ds_read_b128 v[124:127], v242 offset:26944\n"
    "ds_read_b128 v[128:131], v243 offset:64\n"
    "ds_read_b128 v[132:135], v243 offset:26944\n"
    "s_waitcnt vmcnt(25) lgkmcnt(4)\n"
    "v_mfma_f32_16x16x32_bf16 a[0:3], v[0:3], v[104:107], a[0:3]\n"
    "v_mfma_f32_16x16x32_bf16 a[4:7], v[0:3], v[108:111], a[4:7]\n"
    "v_mfma_f32_16x16x32_bf16 a[8:11], v[0:3], v[112:115], a[8:11]\n"
    "v_mfma_f32_16x16x32_bf16 a[12:15], v[0:3], v[116:119], a[12:15]\n"
    "global_load_dwordx4 v[0:3], v241, s[42:43] offset:-4096\n"
    "s_waitcnt vmcnt(25)\n"
    "v_mfma_f32_16x16x32_bf16 a[16:19], v[4:7], v[104:107], a[16:19]\n"
    "v_mfma_f32_16x16x32_bf16 a[20:23], v[4:7], v[108:111], a[20:23]\n"
    "v_mfma_f32_16x16x32_bf16 a[24:27], v[4:7], v[112:115], a[24:27]\n"
    "v_mfma_f32_16x16x32_bf16 a[28:31], v[4:7], v[116:119], a[28:31]\n"
    "global_load_dwordx4 v[4:7], v241, s[42:43] offset:-3072\n"
    "s_waitcnt vmcnt(25)\n"
    "v_mfma_f32_16x16x32_bf16 a[32:35], v[8:11], v[104:107], a[32:35]\n"
    "v_mfma_f32_16x16x32_bf16 a[36:39], v[8:11], v[108:111], a[36:39]\n"
    "v_mfma_f32_16x16x32_bf16 a[40:43], v[8:11], v[112:115], a[40:43]\n"
    "v_mfma_f32_16x16x32_bf16 a[44:47], v[8:11], v[116:119], a[44:47]\n"
    "global_load_dwordx4 v[8:11], v241, s[42:43] offset:-2048\n"
    "s_waitcnt vmcnt(25)\n"
    "v_mfma_f32_16x16x32_bf16 a[48:51], v[12:15], v[104:107], a[48:51]\n"
    "v_mfma_f32_16x16x32_bf16 a[52:55], v[12:15], v[108:111], a[52:55]\n"
    "v_mfma_f32_16x16x32_bf16 a[56:59], v[12:15], v[112:115], a[56:59]\n"
    "v_mfma_f32_16x16x32_bf16 a[60:63], v[12:15], v[116:119], a[60:63]\n"
    "global_load_dwordx4 v[12:15], v241, s[42:43] offset:-1024\n"
    "s_waitcnt vmcnt(25)\n"
    "v_mfma_f32_16x16x32_bf16 a[64:67], v[16:19], v[104:107], a[64:67]\n"
    "v_mfma_f32_16x16x32_bf16 a[68:71], v[16:19], v[108:111], a[68:71]\n"
    "v_mfma_f32_16x16x32_bf16 a[72:75], v[16:19], v[112:115], a[72:75]\n"
    "v_mfma_f32_16x16x32_bf16 a[76:79], v[16:19], v[116:119], a[76:79]\n"
    "global_load_dwordx4 v[16:19], v241, s[42:43] offset:0\n"
    "s_waitcnt vmcnt(25)\n"
    "v_mfma_f32_16x16x32_bf16 a[80:83], v[20:23], v[104:107], a[80:83]\n"
    "v_mfma_f32_16x16x32_bf16 a[84:87], v[20:23], v[108:111], a[84:87]\n"
    "v_mfma_f32_16x16x32_bf16 a[88:91], v[20:23], v[112:115], a[88:91]\n"
    "v_mfma_f32_16x16x32_bf16 a[92:95], v[20:23], v[116:119], a[92:95]\n"
    "global_load_dwordx4 v[20:23], v241, s[42:43] offset:1024\n"
    "s_waitcnt vmcnt(25)\n"
    "v_mfma_f32_16x16x32_bf16 a[96:99], v[24:27], v[104:107], a[96:99]\n"
    "v_mfma_f32_16x16x32_bf16 a[100:103], v[24:27], v[108:111], a[100:103]\n"
    "v_mfma_f32_16x16x32_bf16 a[104:107], v[24:27], v[112:115], a[104:107]\n"
    "v_mfma_f32_16x16x32_bf16 a[108:111], v[24:27], v[116:119], a[108:111]\n"
    "global_load_dwordx4 v[24:27], v241, s[42:43] offset:2048\n"
    "s_waitcnt vmcnt(25)\n"
    "v_mfma_f32_16x16x32_bf16 a[112:115], v[28:31], v[104:107], a[112:115]\n"
    "v_mfma_f32_16x16x32_bf16 a[116:119], v[28:31], v[108:111], a[116:119]\n"
    "v_mfma_f32_16x16x32_bf16 a[120:123], v[28:31], v[112:115], a[120:123]\n"
    "v_mfma_f32_16x16x32_bf16 a[124:127], v[28:31], v[116:119], a[124:127]\n"
    "global_load_dwordx4 v[28:31], v241, s[42:43] offset:3072\n"
    "s_waitcnt vmcnt(25)\n"
    "v_mfma_f32_16x16x32_bf16 a[128:131], v[32:35], v[104:107], a[128:131]\n"
    "v_mfma_f32_16x16x32_bf16 a[132:135], v[32:35], v[108:111], a[132:135]\n"
    "v_mfma_f32_16x16x32_bf16 a[136:139], v[32:35], v[112:115], a[136:139]\n"
    "v_mfma_f32_16x16x32_bf16 a[140:143], v[32:35], v[116:119], a[140:143]\n"
    "global_load_dwordx4 v[32:35], v241, s[44:45] offset:-4096\n"
    "s_waitcnt vmcnt(25)\n"
    "v_mfma_f32_16x16x32_bf16 a[144:147], v[36:39], v[104:107], a[144:147]\n"
    "v_mfma_f32_16x16x32_bf16 a[148:151], v[36:39], v[108:111], a[148:151]\n"
    "v_mfma_f32_16x16x32_bf16 a[152:155], v[36:39], v[112:115], a[152:155]\n"
    "v_mfma_f32_16x16x32_bf16 a[156:159], v[36:39], v[116:119], a[156:159]\n"
    "global_load_dwordx4 v[36:39], v241, s[44:45] offset:-3072\n"
    "s_waitcnt vmcnt(25)\n"
    "v_mfma_f32_16x16x32_bf16 a[160:163], v[40:43], v[104:107], a[160:163]\n"
    "v_mfma_f32_16x16x32_bf16 a[164:167], v[40:43], v[108:111], a[164:167]\n"
    "v_mfma_f32_16x16x32_bf16 a[168:171], v[40:43], v[112:115], a[168:171]\n"
    "v_mfma_f32_16x16x32_bf16 a[172:175], v[40:43], v[116:119], a[172:175]\n"
    "global_load_dwordx4 v[40:43], v241, s[44:45] offset:-2048\n"
    "s_waitcnt vmcnt(25)\n"
    "v_mfma_f32_16x16x32_bf16 a[176:179], v[44:47], v[104:107], a[176:179]\n"
    "v_mfma_f32_16x16x32_bf16 a[180:183], v[44:47], v[108:111], a[180:183]\n"
    "v_mfma_f32_16x16x32_bf16 a[184:187], v[44:47], v[112:115], a[184:187]\n"
    "v_mfma_f32_16x16x32_bf16 a[188:191], v[44:47], v[116:119], a[188:191]\n"
    "global_load_dwordx4 v[44:47], v241, s[44:45] offset:-1024\n"
    "s_waitcnt vmcnt(25)\n"
    "v_mfma_f32_16x16x32_bf16 a[192:195], v[48:51], v[104:107], a[192:195]\n"
    "v_mfma_f32_16x16x32_bf16 a[196:199], v[48:51], v[108:111], a[196:199]\n"
    "v_mfma_f32_16x16x32_bf16 a[200:203], v[48:51], v[112:115], a[200:203]\n"
    "v_mfma_f32_16x16x32_bf16 a[204:207], v[48:51], v[116:119], a[204:207]\n"
    "global_load_dwordx4 v[48:51], v241, s[44:45] offset:0\n"
    "ds_read_b128 v[104:107], v242 offset:128\n"
    "ds_read_b128 v[108:111], v242 offset:27008\n"
    "ds_read_b128 v[112:115], v243 offset:128\n"
    "ds_read_b128 v[116:119], v243 offset:27008\n"
    "s_waitcnt vmcnt(25) lgkmcnt(4)\n"
    "v_mfma_f32_16x16x32_bf16 a[0:3], v[52:55], v[120:123], a[0:3]\n"
    "v_mfma_f32_16x16x32_bf16 a[4:7], v[52:55], v[124:127], a[4:7]\n"
    "v_mfma_f32_16x16x32_bf16 a[8:11], v[52:55], v[128:131], a[8:11]\n"
    "v_mfma_f32_16x16x32_bf16 a[12:15], v[52:55], v[132:135], a[12:15]\n"
    "global_load_dwordx4 v[52:55], v241, s[44:45] offset:1024\n"
    "s_waitcnt vmcnt(25)\n"
    "v_mfma_f32_16x16x32_bf16 a[16:19], v[56:59], v[120:123], a[16:19]\n"
    "v_mfma_f32_16x16x32_bf16 a[20:23], v[56:59], v[124:127], a[20:23]\n"
    "v_mfma_f32_16x16x32_bf16 a[24:27], v[56:59], v[128:131], a[24:27]\n"
    "v_mfma_f32_16x16x32_bf16 a[28:31], v[56:59], v[132:135], a[28:31]\n"
    "global_load_dwordx4 v[56:59], v241, s[44:45] offset:2048\n"
    "s_waitcnt vmcnt(25)\n"
    "v_mfma_f32_16x16x32_bf16 a[32:35], v[60:63], v[120:123], a[32:35]\n"
    "v_mfma_f32_16x16x32_bf16 a[36:39], v[60:63], v[124:127], a[36:39]\n"
    "v_mfma_f32_16x16x32_bf16 a[40:43], v[60:63], v[128:131], a[40:43]\n"
    "v_mfma_f32_16x16x32_bf16 a[44:47], v[60:63], v[132:135], a[44:47]\n"
    "global_load_dwordx4 v[60:63], v241, s[44:45] offset:3072\n"
    "s_waitcnt vmcnt(25)\n"
    "v_mfma_f32_16x16x32_bf16 a[48:51], v[64:67], v[120:123], a[48:51]\n"
    "v_mfma_f32_16x16x32_bf16 a[52:55], v[64:67], v[124:127], a[52:55]\n"
    "v_mfma_f32_16x16x32_bf16 a[56:59], v[64:67], v[128:131], a[56:59]\n"
    "v_mfma_f32_16x16x32_bf16 a[60:63], v[64:67], v[132:135], a[60:63]\n"
    "global_load_dwordx4 v[64:67], v241, s[46:47] offset:-4096\n"
    "s_waitcnt vmcnt(25)\n"
    "v_mfma_f32_16x16x32_bf16 a[64:67], v[68:71], v[120:123], a[64:67]\n"
    "v_mfma_f32_16x16x32_bf16 a[68:71], v[68:71], v[124:127], a[68:71]\n"
    "v_mfma_f32_16x16x32_bf16 a[72:75], v[68:71], v[128:131], a[72:75]\n"
    "v_mfma_f32_16x16x32_bf16 a[76:79], v[68:71], v[132:135], a[76:79]\n"
    "global_load_dwordx4 v[68:71], v241, s[46:47] offset:-3072\n"
    "s_waitcnt vmcnt(25)\n"
    "v_mfma_f32_16x16x32_bf16 a[80:83], v[72:75], v[120:123], a[80:83]\n"
    "v_mfma_f32_16x16x32_bf16 a[84:87], v[72:75], v[124:127], a[84:87]\n"
    "v_mfma_f32_16x16x32_bf16 a[88:91], v[72:75], v[128:131], a[88:91]\n"
    "v_mfma_f32_16x16x32_bf16 a[92:95], v[72:75], v[132:135], a[92:95]\n"
    "global_load_dwordx4 v[72:75], v241, s[46:47] offset:-2048\n"
    "s_waitcnt vmcnt(25)\n"
    "v_mfma_f32_16x16x32_bf16 a[96:99], v[76:79], v[120:123], a[96:99]\n"
    "v_mfma_f32_16x16x32_bf16 a[100:103], v[76:79], v[124:127], a[100:103]\n"
    "v_mfma_f32_16x16x32_bf16 a[104:107], v[76:79], v[128:131], a[104:107]\n"
    "v_mfma_f32_16x16x32_bf16 a[108:111], v[76:79], v[132:135], a[108:111]\n"
    "global_load_dwordx4 v[76:79], v241, s[46:47] offset:-1024\n"
    "s_waitcnt vmcnt(25)\n"
    "v_mfma_f32_16x16x32_bf16 a[112:115], v[80:83], v[120:123], a[112:115]\n"
    "v_mfma_f32_16x16x32_bf16 a[116:119], v[80:83], v[124:127], a[116:119]\n"
    "v_mfma_f32_16x16x32_bf16 a[120:123], v[80:83], v[128:131], a[120:123]\n"
    "v_mfma_f32_16x16x32_bf16 a[124:127], v[80:83], v[132:135], a[124:127]\n"
    "global_load_dwordx4 v[80:83], v241, s[46:47] offset:0\n"
    "s_waitcnt vmcnt(25)\n"
    "v_mfma_f32_16x16x32_bf16 a[128:131], v[84:87], v[120:123], a[128:131]\n"
    "v_mfma_f32_16x16x32_bf16 a[132:135], v[84:87], v[124:127], a[132:135]\n"
    "v_mfma_f32_16x16x32_bf16 a[136:139], v[84:87], v[128:131], a[136:139]\n"
    "v_mfma_f32_16x16x32_bf16 a[140:143], v[84:87], v[132:135], a[140:143]\n"
    "global_load_dwordx4 v[84:87], v241, s[46:47] offset:1024\n"
    "s_waitcnt vmcnt(25)\n"
    "v_mfma_f32_16x16x32_bf16 a[144:147], v[88:91], v[120:123], a[144:147]\n"
    "v_mfma_f32_16x16x32_bf16 a[148:151], v[88:91], v[124:127], a[148:151]\n"
    "v_mfma_f32_16x16x32_bf16 a[152:155], v[88:91], v[128:131], a[152:155]\n"
    "v_mfma_f32_16x16x32_bf16 a[156:159], v[88:91], v[132:135], a[156:159]\n"
    "global_load_dwordx4 v[88:91], v241, s[46:47] offset:2048\n"
    "s_waitcnt vmcnt(25)\n"
    "v_mfma_f32_16x16x32_bf16 a[160:163], v[92:95], v[120:123], a[160:163]\n"
    "v_mfma_f32_16x16x32_bf16 a[164:167], v[92:95], v[124:127], a[164:167]\n"
    "v_mfma_f32_16x16x32_bf16 a[168:171], v[92:95], v[128:131], a[168:171]\n"
    "v_mfma_f32_16x16x32_bf16 a[172:175], v[92:95], v[132:135], a[172:175]\n"
    "global_load_dwordx4 v[92:95], v241, s[46:47] offset:3072\n"
    "s_waitcnt vmcnt(25)\n"
    "v_mfma_f32_16x16x32_bf16 a[176:179], v[96:99], v[120:123], a[176:179]\n"
    "v_mfma_f32_16x16x32_bf16 a[180:183], v[96:99], v[124:127], a[180:183]\n"
    "v_mfma_f32_16x16x32_bf16 a[184:187], v[96:99], v[128:131], a[184:187]\n"
    "v_mfma_f32_16x16x32_bf16 a[188:191], v[96:99], v[132:135], a[188:191]\n"
    "global_load_dwordx4 v[96:99], v241, s[48:49] offset:-4096\n"
    "s_waitcnt vmcnt(25)\n"
    "v_mfma_f32_16x16x32_bf16 a[192:195], v[100:103], v[120:123], a[192:195]\n"
    "v_mfma_f32_16x16x32_bf16 a[196:199], v[100:103], v[124:127], a[196:199]\n"
    "v_mfma_f32_16x16x32_bf16 a[200:203], v[100:103], v[128:131], a[200:203]\n"
    "v_mfma_f32_16x16x32_bf16 a[204:207], v[100:103], v[132:135], a[204:207]\n"
    "global_load_dwordx4 v[100:103], v241, s[48:49] offset:-3072\n"
    "v_add_u32 v242, 128, v242\n"
    "v_add_u32 v243, 128, v243\n"
    "s_sub_u32 s57, s57, 1\n"
    "s_cmp_lg_u32 s57, 0\n"
    "s_cbranch_scc1 L_loop_2_%=\n"
    "s_waitcnt lgkmcnt(0)\n"
    "s_nop 7\n"
    "s_nop 7\n"
    "s_barrier\n"
    "v_lshrrev_b32 v253, 4, v240\n"
    "v_lshlrev_b32 v253, 3, v253\n"
    "s_mul_i32 s58, s56, 416\n"
    "v_add_u32 v253, s58, v253\n"
    "v_and_b32 v254, 15, v240\n"
    "v_mul_u32_u24 v255, 1664, v254\n"
    "v_add_u32 v255, v255, v253\n"
    "v_mul_u32_u24 v254, 1680, v254\n"
    "v_add_u32 v254, v254, v253\n"
    "ds_read_b64 v[104:105], v254 offset:0\n"
    "ds_read_b64 v[106:107], v254 offset:32\n"
    "ds_read_b64 v[108:109], v254 offset:64\n"
    "ds_read_b64 v[110:111], v254 offset:96\n"
    "ds_read_b64 v[112:113], v254 offset:128\n"
    "ds_read_b64 v[114:115], v254 offset:160\n"
    "ds_read_b64 v[116:117], v254 offset:192\n"
    "ds_read_b64 v[118:119], v254 offset:224\n"
    "ds_read_b64 v[120:121], v254 offset:256\n"
    "ds_read_b64 v[122:123], v254 offset:288\n"
    "ds_read_b64 v[124:125], v254 offset:320\n"
    "ds_read_b64 v[126:127], v254 offset:352\n"
    "ds_read_b64 v[128:129], v254 offset:384\n"
    "s_add_u32 s58, s54, 0\n"
    "s_addc_u32 s59, s55, 0\n"
    "s_waitcnt lgkmcnt(12)\n"
    "v_accvgpr_read_b32 v244, a0\n"
    "v_accvgpr_read_b32 v245, a1\n"
    "v_accvgpr_read_b32 v246, a2\n"
    "v_accvgpr_read_b32 v247, a3\n"
    "v_lshlrev_b32 v248, 16, v136\n"
    "v_lshlrev_b32 v249, 16, v104\n"
    "v_fmac_f32 v249, v248, v244\n"
    "v_and_b32 v248, 0xffff0000, v136\n"
    "v_and_b32 v250, 0xffff0000, v104\n"
    "v_fmac_f32 v250, v248, v245\n"
    "v_cvt_pk_bf16_f32 v104, v249, v250\n"
    "v_lshlrev_b32 v248, 16, v137\n"
    "v_lshlrev_b32 v249, 16, v105\n"
    "v_fmac_f32 v249, v248, v246\n"
    "v_and_b32 v248, 0xffff0000, v137\n"
    "v_and_b32 v250, 0xffff0000, v105\n"
    "v_fmac_f32 v250, v248, v247\n"
    "v_cvt_pk_bf16_f32 v105, v249, v250\n"
    "s_waitcnt lgkmcnt(11)\n"
    "v_accvgpr_read_b32 v244, a16\n"
    "v_accvgpr_read_b32 v245, a17\n"
    "v_accvgpr_read_b32 v246, a18\n"
    "v_accvgpr_read_b32 v247, a19\n"
    "v_lshlrev_b32 v248, 16, v138\n"
    "v_lshlrev_b32 v249, 16, v106\n"
    "v_fmac_f32 v249, v248, v244\n"
    "v_and_b32 v248, 0xffff0000, v138\n"
    "v_and_b32 v250, 0xffff0000, v106\n"
    "v_fmac_f32 v250, v248, v245\n"
    "v_cvt_pk_bf16_f32 v106, v249, v250\n"
    "v_lshlrev_b32 v248, 16, v139\n"
    "v_lshlrev_b32 v249, 16, v107\n"
    "v_fmac_f32 v249, v248, v246\n"
    "v_and_b32 v248, 0xffff0000, v139\n"
    "v_and_b32 v250, 0xffff0000, v107\n"
    "v_fmac_f32 v250, v248, v247\n"
    "v_cvt_pk_bf16_f32 v107, v249, v250\n"
    "s_waitcnt lgkmcnt(10)\n"
    "v_accvgpr_read_b32 v244, a32\n"
    "v_accvgpr_read_b32 v245, a33\n"
    "v_accvgpr_read_b32 v246, a34\n"
    "v_accvgpr_read_b32 v247, a35\n"
    "v_lshlrev_b32 v248, 16, v140\n"
    "v_lshlrev_b32 v249, 16, v108\n"
    "v_fmac_f32 v249, v248, v244\n"
    "v_and_b32 v248, 0xffff0000, v140\n"
    "v_and_b32 v250, 0xffff0000, v108\n"
    "v_fmac_f32 v250, v248, v245\n"
    "v_cvt_pk_bf16_f32 v108, v249, v250\n"
    "v_lshlrev_b32 v248, 16, v141\n"
    "v_lshlrev_b32 v249, 16, v109\n"
    "v_fmac_f32 v249, v248, v246\n"
    "v_and_b32 v248, 0xffff0000, v141\n"
    "v_and_b32 v250, 0xffff0000, v109\n"
    "v_fmac_f32 v250, v248, v247\n"
    "v_cvt_pk_bf16_f32 v109, v249, v250\n"
    "s_waitcnt lgkmcnt(9)\n"
    "v_accvgpr_read_b32 v244, a48\n"
    "v_accvgpr_read_b32 v245, a49\n"
    "v_accvgpr_read_b32 v246, a50\n"
    "v_accvgpr_read_b32 v247, a51\n"
    "v_lshlrev_b32 v248, 16, v142\n"
    "v_lshlrev_b32 v249, 16, v110\n"
    "v_fmac_f32 v249, v248, v244\n"
    "v_and_b32 v248, 0xffff0000, v142\n"
    "v_and_b32 v250, 0xffff0000, v110\n"
    "v_fmac_f32 v250, v248, v245\n"
    "v_cvt_pk_bf16_f32 v110, v249, v250\n"
    "v_lshlrev_b32 v248, 16, v143\n"
    "v_lshlrev_b32 v249, 16, v111\n"
    "v_fmac_f32 v249, v248, v246\n"
    "v_and_b32 v248, 0xffff0000, v143\n"
    "v_and_b32 v250, 0xffff0000, v111\n"
    "v_fmac_f32 v250, v248, v247\n"
    "v_cvt_pk_bf16_f32 v111, v249, v250\n"
    "s_waitcnt lgkmcnt(8)\n"
    "v_accvgpr_read_b32 v244, a64\n"
    "v_accvgpr_read_b32 v245, a65\n"
    "v_accvgpr_read_b32 v246, a66\n"
    "v_accvgpr_read_b32 v247, a67\n"
    "v_lshlrev_b32 v248, 16, v144\n"
    "v_lshlrev_b32 v249, 16, v112\n"
    "v_fmac_f32 v249, v248, v244\n"
    "v_and_b32 v248, 0xffff0000, v144\n"
    "v_and_b32 v250, 0xffff0000, v112\n"
    "v_fmac_f32 v250, v248, v245\n"
    "v_cvt_pk_bf16_f32 v112, v249, v250\n"
    "v_lshlrev_b32 v248, 16, v145\n"
    "v_lshlrev_b32 v249, 16, v113\n"
    "v_fmac_f32 v249, v248, v246\n"
    "v_and_b32 v248, 0xffff0000, v145\n"
    "v_and_b32 v250, 0xffff0000, v113\n"
    "v_fmac_f32 v250, v248, v247\n"
    "v_cvt_pk_bf16_f32 v113, v249, v250\n"
    "s_waitcnt lgkmcnt(7)\n"
    "v_accvgpr_read_b32 v244, a80\n"
    "v_accvgpr_read_b32 v245, a81\n"
    "v_accvgpr_read_b32 v246, a82\n"
    "v_accvgpr_read_b32 v247, a83\n"
    "v_lshlrev_b32 v248, 16, v146\n"
    "v_lshlrev_b32 v249, 16, v114\n"
    "v_fmac_f32 v249, v248, v244\n"
    "v_and_b32 v248, 0xffff0000, v146\n"
    "v_and_b32 v250, 0xffff0000, v114\n"
    "v_fmac_f32 v250, v248, v245\n"
    "v_cvt_pk_bf16_f32 v114, v249, v250\n"
    "v_lshlrev_b32 v248, 16, v147\n"
    "v_lshlrev_b32 v249, 16, v115\n"
    "v_fmac_f32 v249, v248, v246\n"
    "v_and_b32 v248, 0xffff0000, v147\n"
    "v_and_b32 v250, 0xffff0000, v115\n"
    "v_fmac_f32 v250, v248, v247\n"
    "v_cvt_pk_bf16_f32 v115, v249, v250\n"
    "s_waitcnt lgkmcnt(6)\n"
    "v_accvgpr_read_b32 v244, a96\n"
    "v_accvgpr_read_b32 v245, a97\n"
    "v_accvgpr_read_b32 v246, a98\n"
    "v_accvgpr_read_b32 v247, a99\n"
    "v_lshlrev_b32 v248, 16, v148\n"
    "v_lshlrev_b32 v249, 16, v116\n"
    "v_fmac_f32 v249, v248, v244\n"
    "v_and_b32 v248, 0xffff0000, v148\n"
    "v_and_b32 v250, 0xffff0000, v116\n"
    "v_fmac_f32 v250, v248, v245\n"
    "v_cvt_pk_bf16_f32 v116, v249, v250\n"
    "v_lshlrev_b32 v248, 16, v149\n"
    "v_lshlrev_b32 v249, 16, v117\n"
    "v_fmac_f32 v249, v248, v246\n"
    "v_and_b32 v248, 0xffff0000, v149\n"
    "v_and_b32 v250, 0xffff0000, v117\n"
    "v_fmac_f32 v250, v248, v247\n"
    "v_cvt_pk_bf16_f32 v117, v249, v250\n"
    "s_waitcnt lgkmcnt(5)\n"
    "v_accvgpr_read_b32 v244, a112\n"
    "v_accvgpr_read_b32 v245, a113\n"
    "v_accvgpr_read_b32 v246, a114\n"
    "v_accvgpr_read_b32 v247, a115\n"
    "v_lshlrev_b32 v248, 16, v150\n"
    "v_lshlrev_b32 v249, 16, v118\n"
    "v_fmac_f32 v249, v248, v244\n"
    "v_and_b32 v248, 0xffff0000, v150\n"
    "v_and_b32 v250, 0xffff0000, v118\n"
    "v_fmac_f32 v250, v248, v245\n"
    "v_cvt_pk_bf16_f32 v118, v249, v250\n"
    "v_lshlrev_b32 v248, 16, v151\n"
    "v_lshlrev_b32 v249, 16, v119\n"
    "v_fmac_f32 v249, v248, v246\n"
    "v_and_b32 v248, 0xffff0000, v151\n"
    "v_and_b32 v250, 0xffff0000, v119\n"
    "v_fmac_f32 v250, v248, v247\n"
    "v_cvt_pk_bf16_f32 v119, v249, v250\n"
    "s_waitcnt lgkmcnt(4)\n"
    "v_accvgpr_read_b32 v244, a128\n"
    "v_accvgpr_read_b32 v245, a129\n"
    "v_accvgpr_read_b32 v246, a130\n"
    "v_accvgpr_read_b32 v247, a131\n"
    "v_lshlrev_b32 v248, 16, v152\n"
    "v_lshlrev_b32 v249, 16, v120\n"
    "v_fmac_f32 v249, v248, v244\n"
    "v_and_b32 v248, 0xffff0000, v152\n"
    "v_and_b32 v250, 0xffff0000, v120\n"
    "v_fmac_f32 v250, v248, v245\n"
    "v_cvt_pk_bf16_f32 v120, v249, v250\n"
    "v_lshlrev_b32 v248, 16, v153\n"
    "v_lshlrev_b32 v249, 16, v121\n"
    "v_fmac_f32 v249, v248, v246\n"
    "v_and_b32 v248, 0xffff0000, v153\n"
    "v_and_b32 v250, 0xffff0000, v121\n"
    "v_fmac_f32 v250, v248, v247\n"
    "v_cvt_pk_bf16_f32 v121, v249, v250\n"
    "s_waitcnt lgkmcnt(3)\n"
    "v_accvgpr_read_b32 v244, a144\n"
    "v_accvgpr_read_b32 v245, a145\n"
    "v_accvgpr_read_b32 v246, a146\n"
    "v_accvgpr_read_b32 v247, a147\n"
    "v_lshlrev_b32 v248, 16, v154\n"
    "v_lshlrev_b32 v249, 16, v122\n"
    "v_fmac_f32 v249, v248, v244\n"
    "v_and_b32 v248, 0xffff0000, v154\n"
    "v_and_b32 v250, 0xffff0000, v122\n"
    "v_fmac_f32 v250, v248, v245\n"
    "v_cvt_pk_bf16_f32 v122, v249, v250\n"
    "v_lshlrev_b32 v248, 16, v155\n"
    "v_lshlrev_b32 v249, 16, v123\n"
    "v_fmac_f32 v249, v248, v246\n"
    "v_and_b32 v248, 0xffff0000, v155\n"
    "v_and_b32 v250, 0xffff0000, v123\n"
    "v_fmac_f32 v250, v248, v247\n"
    "v_cvt_pk_bf16_f32 v123, v249, v250\n"
    "s_waitcnt lgkmcnt(2)\n"
    "v_accvgpr_read_b32 v244, a160\n"
    "v_accvgpr_read_b32 v245, a161\n"
    "v_accvgpr_read_b32 v246, a162\n"
    "v_accvgpr_read_b32 v247, a163\n"
    "v_lshlrev_b32 v248, 16, v156\n"
    "v_lshlrev_b32 v249, 16, v124\n"
    "v_fmac_f32 v249, v248, v244\n"
    "v_and_b32 v248, 0xffff0000, v156\n"
    "v_and_b32 v250, 0xffff0000, v124\n"
    "v_fmac_f32 v250, v248, v245\n"
    "v_cvt_pk_bf16_f32 v124, v249, v250\n"
    "v_lshlrev_b32 v248, 16, v157\n"
    "v_lshlrev_b32 v249, 16, v125\n"
    "v_fmac_f32 v249, v248, v246\n"
    "v_and_b32 v248, 0xffff0000, v157\n"
    "v_and_b32 v250, 0xffff0000, v125\n"
    "v_fmac_f32 v250, v248, v247\n"
    "v_cvt_pk_bf16_f32 v125, v249, v250\n"
    "s_waitcnt lgkmcnt(1)\n"
    "v_accvgpr_read_b32 v244, a176\n"
    "v_accvgpr_read_b32 v245, a177\n"
    "v_accvgpr_read_b32 v246, a178\n"
    "v_accvgpr_read_b32 v247, a179\n"
    "v_lshlrev_b32 v248, 16, v158\n"
    "v_lshlrev_b32 v249, 16, v126\n"
    "v_fmac_f32 v249, v248, v244\n"
    "v_and_b32 v248, 0xffff0000, v158\n"
    "v_and_b32 v250, 0xffff0000, v126\n"
    "v_fmac_f32 v250, v248, v245\n"
    "v_cvt_pk_bf16_f32 v126, v249, v250\n"
    "v_lshlrev_b32 v248, 16, v159\n"
    "v_lshlrev_b32 v249, 16, v127\n"
    "v_fmac_f32 v249, v248, v246\n"
    "v_and_b32 v248, 0xffff0000, v159\n"
    "v_and_b32 v250, 0xffff0000, v127\n"
    "v_fmac_f32 v250, v248, v247\n"
    "v_cvt_pk_bf16_f32 v127, v249, v250\n"
    "s_waitcnt lgkmcnt(0)\n"
    "v_accvgpr_read_b32 v244, a192\n"
    "v_accvgpr_read_b32 v245, a193\n"
    "v_accvgpr_read_b32 v246, a194\n"
    "v_accvgpr_read_b32 v247, a195\n"
    "v_lshlrev_b32 v248, 16, v160\n"
    "v_lshlrev_b32 v249, 16, v128\n"
    "v_fmac_f32 v249, v248, v244\n"
    "v_and_b32 v248, 0xffff0000, v160\n"
    "v_and_b32 v250, 0xffff0000, v128\n"
    "v_fmac_f32 v250, v248, v245\n"
    "v_cvt_pk_bf16_f32 v128, v249, v250\n"
    "v_lshlrev_b32 v248, 16, v161\n"
    "v_lshlrev_b32 v249, 16, v129\n"
    "v_fmac_f32 v249, v248, v246\n"
    "v_and_b32 v248, 0xffff0000, v161\n"
    "v_and_b32 v250, 0xffff0000, v129\n"
    "v_fmac_f32 v250, v248, v247\n"
    "v_cvt_pk_bf16_f32 v129, v249, v250\n"
    "global_store_dwordx2 v255, v[104:105], s[58:59] offset:0\n"
    "global_store_dwordx2 v255, v[106:107], s[58:59] offset:32\n"
    "global_store_dwordx2 v255, v[108:109], s[58:59] offset:64\n"
    "global_store_dwordx2 v255, v[110:111], s[58:59] offset:96\n"
    "global_store_dwordx2 v255, v[112:113], s[58:59] offset:128\n"
    "global_store_dwordx2 v255, v[114:115], s[58:59] offset:160\n"
    "global_store_dwordx2 v255, v[116:117], s[58:59] offset:192\n"
    "global_store_dwordx2 v255, v[118:119], s[58:59] offset:224\n"
    "global_store_dwordx2 v255, v[120:121], s[58:59] offset:256\n"
    "global_store_dwordx2 v255, v[122:123], s[58:59] offset:288\n"
    "global_store_dwordx2 v255, v[124:125], s[58:59] offset:320\n"
    "global_store_dwordx2 v255, v[126:127], s[58:59] offset:352\n"
    "global_store_dwordx2 v255, v[128:129], s[58:59] offset:384\n"
    "ds_read_b64 v[104:105], v254 offset:26880\n"
    "ds_read_b64 v[106:107], v254 offset:26912\n"
    "ds_read_b64 v[108:109], v254 offset:26944\n"
    "ds_read_b64 v[110:111], v254 offset:26976\n"
    "ds_read_b64 v[112:113], v254 offset:27008\n"
    "ds_read_b64 v[114:115], v254 offset:27040\n"
    "ds_read_b64 v[116:117], v254 offset:27072\n"
    "ds_read_b64 v[118:119], v254 offset:27104\n"
    "ds_read_b64 v[120:121], v254 offset:27136\n"
    "ds_read_b64 v[122:123], v254 offset:27168\n"
    "ds_read_b64 v[124:125], v254 offset:27200\n"
    "ds_read_b64 v[126:127], v254 offset:27232\n"
    "ds_read_b64 v[128:129], v254 offset:27264\n"
    "s_add_u32 s58, s54, 26624\n"
    "s_addc_u32 s59, s55, 0\n"
    "s_waitcnt lgkmcnt(12)\n"
    "v_accvgpr_read_b32 v244, a4\n"
    "v_accvgpr_read_b32 v245, a5\n"
    "v_accvgpr_read_b32 v246, a6\n"
    "v_accvgpr_read_b32 v247, a7\n"
    "v_lshlrev_b32 v248, 16, v162\n"
    "v_lshlrev_b32 v249, 16, v104\n"
    "v_fmac_f32 v249, v248, v244\n"
    "v_and_b32 v248, 0xffff0000, v162\n"
    "v_and_b32 v250, 0xffff0000, v104\n"
    "v_fmac_f32 v250, v248, v245\n"
    "v_cvt_pk_bf16_f32 v104, v249, v250\n"
    "v_lshlrev_b32 v248, 16, v163\n"
    "v_lshlrev_b32 v249, 16, v105\n"
    "v_fmac_f32 v249, v248, v246\n"
    "v_and_b32 v248, 0xffff0000, v163\n"
    "v_and_b32 v250, 0xffff0000, v105\n"
    "v_fmac_f32 v250, v248, v247\n"
    "v_cvt_pk_bf16_f32 v105, v249, v250\n"
    "s_waitcnt lgkmcnt(11)\n"
    "v_accvgpr_read_b32 v244, a20\n"
    "v_accvgpr_read_b32 v245, a21\n"
    "v_accvgpr_read_b32 v246, a22\n"
    "v_accvgpr_read_b32 v247, a23\n"
    "v_lshlrev_b32 v248, 16, v164\n"
    "v_lshlrev_b32 v249, 16, v106\n"
    "v_fmac_f32 v249, v248, v244\n"
    "v_and_b32 v248, 0xffff0000, v164\n"
    "v_and_b32 v250, 0xffff0000, v106\n"
    "v_fmac_f32 v250, v248, v245\n"
    "v_cvt_pk_bf16_f32 v106, v249, v250\n"
    "v_lshlrev_b32 v248, 16, v165\n"
    "v_lshlrev_b32 v249, 16, v107\n"
    "v_fmac_f32 v249, v248, v246\n"
    "v_and_b32 v248, 0xffff0000, v165\n"
    "v_and_b32 v250, 0xffff0000, v107\n"
    "v_fmac_f32 v250, v248, v247\n"
    "v_cvt_pk_bf16_f32 v107, v249, v250\n"
    "s_waitcnt lgkmcnt(10)\n"
    "v_accvgpr_read_b32 v244, a36\n"
    "v_accvgpr_read_b32 v245, a37\n"
    "v_accvgpr_read_b32 v246, a38\n"
    "v_accvgpr_read_b32 v247, a39\n"
    "v_lshlrev_b32 v248, 16, v166\n"
    "v_lshlrev_b32 v249, 16, v108\n"
    "v_fmac_f32 v249, v248, v244\n"
    "v_and_b32 v248, 0xffff0000, v166\n"
    "v_and_b32 v250, 0xffff0000, v108\n"
    "v_fmac_f32 v250, v248, v245\n"
    "v_cvt_pk_bf16_f32 v108, v249, v250\n"
    "v_lshlrev_b32 v248, 16, v167\n"
    "v_lshlrev_b32 v249, 16, v109\n"
    "v_fmac_f32 v249, v248, v246\n"
    "v_and_b32 v248, 0xffff0000, v167\n"
    "v_and_b32 v250, 0xffff0000, v109\n"
    "v_fmac_f32 v250, v248, v247\n"
    "v_cvt_pk_bf16_f32 v109, v249, v250\n"
    "s_waitcnt lgkmcnt(9)\n"
    "v_accvgpr_read_b32 v244, a52\n"
    "v_accvgpr_read_b32 v245, a53\n"
    "v_accvgpr_read_b32 v246, a54\n"
    "v_accvgpr_read_b32 v247, a55\n"
    "v_lshlrev_b32 v248, 16, v168\n"
    "v_lshlrev_b32 v249, 16, v110\n"
    "v_fmac_f32 v249, v248, v244\n"
    "v_and_b32 v248, 0xffff0000, v168\n"
    "v_and_b32 v250, 0xffff0000, v110\n"
    "v_fmac_f32 v250, v248, v245\n"
    "v_cvt_pk_bf16_f32 v110, v249, v250\n"
    "v_lshlrev_b32 v248, 16, v169\n"
    "v_lshlrev_b32 v249, 16, v111\n"
    "v_fmac_f32 v249, v248, v246\n"
    "v_and_b32 v248, 0xffff0000, v169\n"
    "v_and_b32 v250, 0xffff0000, v111\n"
    "v_fmac_f32 v250, v248, v247\n"
    "v_cvt_pk_bf16_f32 v111, v249, v250\n"
    "s_waitcnt lgkmcnt(8)\n"
    "v_accvgpr_read_b32 v244, a68\n"
    "v_accvgpr_read_b32 v245, a69\n"
    "v_accvgpr_read_b32 v246, a70\n"
    "v_accvgpr_read_b32 v247, a71\n"
    "v_lshlrev_b32 v248, 16, v170\n"
    "v_lshlrev_b32 v249, 16, v112\n"
    "v_fmac_f32 v249, v248, v244\n"
    "v_and_b32 v248, 0xffff0000, v170\n"
    "v_and_b32 v250, 0xffff0000, v112\n"
    "v_fmac_f32 v250, v248, v245\n"
    "v_cvt_pk_bf16_f32 v112, v249, v250\n"
    "v_lshlrev_b32 v248, 16, v171\n"
    "v_lshlrev_b32 v249, 16, v113\n"
    "v_fmac_f32 v249, v248, v246\n"
    "v_and_b32 v248, 0xffff0000, v171\n"
    "v_and_b32 v250, 0xffff0000, v113\n"
    "v_fmac_f32 v250, v248, v247\n"
    "v_cvt_pk_bf16_f32 v113, v249, v250\n"
    "s_waitcnt lgkmcnt(7)\n"
    "v_accvgpr_read_b32 v244, a84\n"
    "v_accvgpr_read_b32 v245, a85\n"
    "v_accvgpr_read_b32 v246, a86\n"
    "v_accvgpr_read_b32 v247, a87\n"
    "v_lshlrev_b32 v248, 16, v172\n"
    "v_lshlrev_b32 v249, 16, v114\n"
    "v_fmac_f32 v249, v248, v244\n"
    "v_and_b32 v248, 0xffff0000, v172\n"
    "v_and_b32 v250, 0xffff0000, v114\n"
    "v_fmac_f32 v250, v248, v245\n"
    "v_cvt_pk_bf16_f32 v114, v249, v250\n"
    "v_lshlrev_b32 v248, 16, v173\n"
    "v_lshlrev_b32 v249, 16, v115\n"
    "v_fmac_f32 v249, v248, v246\n"
    "v_and_b32 v248, 0xffff0000, v173\n"
    "v_and_b32 v250, 0xffff0000, v115\n"
    "v_fmac_f32 v250, v248, v247\n"
    "v_cvt_pk_bf16_f32 v115, v249, v250\n"
    "s_waitcnt lgkmcnt(6)\n"
    "v_accvgpr_read_b32 v244, a100\n"
    "v_accvgpr_read_b32 v245, a101\n"
    "v_accvgpr_read_b32 v246, a102\n"
    "v_accvgpr_read_b32 v247, a103\n"
    "v_lshlrev_b32 v248, 16, v174\n"
    "v_lshlrev_b32 v249, 16, v116\n"
    "v_fmac_f32 v249, v248, v244\n"
    "v_and_b32 v248, 0xffff0000, v174\n"
    "v_and_b32 v250, 0xffff0000, v116\n"
    "v_fmac_f32 v250, v248, v245\n"
    "v_cvt_pk_bf16_f32 v116, v249, v250\n"
    "v_lshlrev_b32 v248, 16, v175\n"
    "v_lshlrev_b32 v249, 16, v117\n"
    "v_fmac_f32 v249, v248, v246\n"
    "v_and_b32 v248, 0xffff0000, v175\n"
    "v_and_b32 v250, 0xffff0000, v117\n"
    "v_fmac_f32 v250, v248, v247\n"
    "v_cvt_pk_bf16_f32 v117, v249, v250\n"
    "s_waitcnt lgkmcnt(5)\n"
    "v_accvgpr_read_b32 v244, a116\n"
    "v_accvgpr_read_b32 v245, a117\n"
    "v_accvgpr_read_b32 v246, a118\n"
    "v_accvgpr_read_b32 v247, a119\n"
    "v_lshlrev_b32 v248, 16, v176\n"
    "v_lshlrev_b32 v249, 16, v118\n"
    "v_fmac_f32 v249, v248, v244\n"
    "v_and_b32 v248, 0xffff0000, v176\n"
    "v_and_b32 v250, 0xffff0000, v118\n"
    "v_fmac_f32 v250, v248, v245\n"
    "v_cvt_pk_bf16_f32 v118, v249, v250\n"
    "v_lshlrev_b32 v248, 16, v177\n"
    "v_lshlrev_b32 v249, 16, v119\n"
    "v_fmac_f32 v249, v248, v246\n"
    "v_and_b32 v248, 0xffff0000, v177\n"
    "v_and_b32 v250, 0xffff0000, v119\n"
    "v_fmac_f32 v250, v248, v247\n"
    "v_cvt_pk_bf16_f32 v119, v249, v250\n"
    "s_waitcnt lgkmcnt(4)\n"
    "v_accvgpr_read_b32 v244, a132\n"
    "v_accvgpr_read_b32 v245, a133\n"
    "v_accvgpr_read_b32 v246, a134\n"
    "v_accvgpr_read_b32 v247, a135\n"
    "v_lshlrev_b32 v248, 16, v178\n"
    "v_lshlrev_b32 v249, 16, v120\n"
    "v_fmac_f32 v249, v248, v244\n"
    "v_and_b32 v248, 0xffff0000, v178\n"
    "v_and_b32 v250, 0xffff0000, v120\n"
    "v_fmac_f32 v250, v248, v245\n"
    "v_cvt_pk_bf16_f32 v120, v249, v250\n"
    "v_lshlrev_b32 v248, 16, v179\n"
    "v_lshlrev_b32 v249, 16, v121\n"
    "v_fmac_f32 v249, v248, v246\n"
    "v_and_b32 v248, 0xffff0000, v179\n"
    "v_and_b32 v250, 0xffff0000, v121\n"
    "v_fmac_f32 v250, v248, v247\n"
    "v_cvt_pk_bf16_f32 v121, v249, v250\n"
    "s_waitcnt lgkmcnt(3)\n"
    "v_accvgpr_read_b32 v244, a148\n"
    "v_accvgpr_read_b32 v245, a149\n"
    "v_accvgpr_read_b32 v246, a150\n"
    "v_accvgpr_read_b32 v247, a151\n"
    "v_lshlrev_b32 v248, 16, v180\n"
    "v_lshlrev_b32 v249, 16, v122\n"
    "v_fmac_f32 v249, v248, v244\n"
    "v_and_b32 v248, 0xffff0000, v180\n"
    "v_and_b32 v250, 0xffff0000, v122\n"
    "v_fmac_f32 v250, v248, v245\n"
    "v_cvt_pk_bf16_f32 v122, v249, v250\n"
    "v_lshlrev_b32 v248, 16, v181\n"
    "v_lshlrev_b32 v249, 16, v123\n"
    "v_fmac_f32 v249, v248, v246\n"
    "v_and_b32 v248, 0xffff0000, v181\n"
    "v_and_b32 v250, 0xffff0000, v123\n"
    "v_fmac_f32 v250, v248, v247\n"
    "v_cvt_pk_bf16_f32 v123, v249, v250\n"
    "s_waitcnt lgkmcnt(2)\n"
    "v_accvgpr_read_b32 v244, a164\n"
    "v_accvgpr_read_b32 v245, a165\n"
    "v_accvgpr_read_b32 v246, a166\n"
    "v_accvgpr_read_b32 v247, a167\n"
    "v_lshlrev_b32 v248, 16, v182\n"
    "v_lshlrev_b32 v249, 16, v124\n"
    "v_fmac_f32 v249, v248, v244\n"
    "v_and_b32 v248, 0xffff0000, v182\n"
    "v_and_b32 v250, 0xffff0000, v124\n"
    "v_fmac_f32 v250, v248, v245\n"
    "v_cvt_pk_bf16_f32 v124, v249, v250\n"
    "v_lshlrev_b32 v248, 16, v183\n"
    "v_lshlrev_b32 v249, 16, v125\n"
    "v_fmac_f32 v249, v248, v246\n"
    "v_and_b32 v248, 0xffff0000, v183\n"
    "v_and_b32 v250, 0xffff0000, v125\n"
    "v_fmac_f32 v250, v248, v247\n"
    "v_cvt_pk_bf16_f32 v125, v249, v250\n"
    "s_waitcnt lgkmcnt(1)\n"
    "v_accvgpr_read_b32 v244, a180\n"
    "v_accvgpr_read_b32 v245, a181\n"
    "v_accvgpr_read_b32 v246, a182\n"
    "v_accvgpr_read_b32 v247, a183\n"
    "v_lshlrev_b32 v248, 16, v184\n"
    "v_lshlrev_b32 v249, 16, v126\n"
    "v_fmac_f32 v249, v248, v244\n"
    "v_and_b32 v248, 0xffff0000, v184\n"
    "v_and_b32 v250, 0xffff0000, v126\n"
    "v_fmac_f32 v250, v248, v245\n"
    "v_cvt_pk_bf16_f32 v126, v249, v250\n"
    "v_lshlrev_b32 v248, 16, v185\n"
    "v_lshlrev_b32 v249, 16, v127\n"
    "v_fmac_f32 v249, v248, v246\n"
    "v_and_b32 v248, 0xffff0000, v185\n"
    "v_and_b32 v250, 0xffff0000, v127\n"
    "v_fmac_f32 v250, v248, v247\n"
    "v_cvt_pk_bf16_f32 v127, v249, v250\n"
    "s_waitcnt lgkmcnt(0)\n"
    "v_accvgpr_read_b32 v244, a196\n"
    "v_accvgpr_read_b32 v245, a197\n"
    "v_accvgpr_read_b32 v246, a198\n"
    "v_accvgpr_read_b32 v247, a199\n"
    "v_lshlrev_b32 v248, 16, v186\n"
    "v_lshlrev_b32 v249, 16, v128\n"
    "v_fmac_f32 v249, v248, v244\n"
    "v_and_b32 v248, 0xffff0000, v186\n"
    "v_and_b32 v250, 0xffff0000, v128\n"
    "v_fmac_f32 v250, v248, v245\n"
    "v_cvt_pk_bf16_f32 v128, v249, v250\n"
    "v_lshlrev_b32 v248, 16, v187\n"
    "v_lshlrev_b32 v249, 16, v129\n"
    "v_fmac_f32 v249, v248, v246\n"
    "v_and_b32 v248, 0xffff0000, v187\n"
    "v_and_b32 v250, 0xffff0000, v129\n"
    "v_fmac_f32 v250, v248, v247\n"
    "v_cvt_pk_bf16_f32 v129, v249, v250\n"
    "global_store_dwordx2 v255, v[104:105], s[58:59] offset:0\n"
    "global_store_dwordx2 v255, v[106:107], s[58:59] offset:32\n"
    "global_store_dwordx2 v255, v[108:109], s[58:59] offset:64\n"
    "global_store_dwordx2 v255, v[110:111], s[58:59] offset:96\n"
    "global_store_dwordx2 v255, v[112:113], s[58:59] offset:128\n"
    "global_store_dwordx2 v255, v[114:115], s[58:59] offset:160\n"
    "global_store_dwordx2 v255, v[116:117], s[58:59] offset:192\n"
    "global_store_dwordx2 v255, v[118:119], s[58:59] offset:224\n"
    "global_store_dwordx2 v255, v[120:121], s[58:59] offset:256\n"
    "global_store_dwordx2 v255, v[122:123], s[58:59] offset:288\n"
    "global_store_dwordx2 v255, v[124:125], s[58:59] offset:320\n"
    "global_store_dwordx2 v255, v[126:127], s[58:59] offset:352\n"
    "global_store_dwordx2 v255, v[128:129], s[58:59] offset:384\n"
    "ds_read_b64 v[104:105], v254 offset:53760\n"
    "ds_read_b64 v[106:107], v254 offset:53792\n"
    "ds_read_b64 v[108:109], v254 offset:53824\n"
    "ds_read_b64 v[110:111], v254 offset:53856\n"
    "ds_read_b64 v[112:113], v254 offset:53888\n"
    "ds_read_b64 v[114:115], v254 offset:53920\n"
    "ds_read_b64 v[116:117], v254 offset:53952\n"
    "ds_read_b64 v[118:119], v254 offset:53984\n"
    "ds_read_b64 v[120:121], v254 offset:54016\n"
    "ds_read_b64 v[122:123], v254 offset:54048\n"
    "ds_read_b64 v[124:125], v254 offset:54080\n"
    "ds_read_b64 v[126:127], v254 offset:54112\n"
    "ds_read_b64 v[128:129], v254 offset:54144\n"
    "s_add_u32 s58, s54, 53248\n"
    "s_addc_u32 s59, s55, 0\n"
    "s_waitcnt lgkmcnt(12)\n"
    "v_accvgpr_read_b32 v244, a8\n"
    "v_accvgpr_read_b32 v245, a9\n"
    "v_accvgpr_read_b32 v246, a10\n"
    "v_accvgpr_read_b32 v247, a11\n"
    "v_lshlrev_b32 v248, 16, v188\n"
    "v_lshlrev_b32 v249, 16, v104\n"
    "v_fmac_f32 v249, v248, v244\n"
    "v_and_b32 v248, 0xffff0000, v188\n"
    "v_and_b32 v250, 0xffff0000, v104\n"
    "v_fmac_f32 v250, v248, v245\n"
    "v_cvt_pk_bf16_f32 v104, v249, v250\n"
    "v_lshlrev_b32 v248, 16, v189\n"
    "v_lshlrev_b32 v249, 16, v105\n"
    "v_fmac_f32 v249, v248, v246\n"
    "v_and_b32 v248, 0xffff0000, v189\n"
    "v_and_b32 v250, 0xffff0000, v105\n"
    "v_fmac_f32 v250, v248, v247\n"
    "v_cvt_pk_bf16_f32 v105, v249, v250\n"
    "s_waitcnt lgkmcnt(11)\n"
    "v_accvgpr_read_b32 v244, a24\n"
    "v_accvgpr_read_b32 v245, a25\n"
    "v_accvgpr_read_b32 v246, a26\n"
    "v_accvgpr_read_b32 v247, a27\n"
    "v_lshlrev_b32 v248, 16, v190\n"
    "v_lshlrev_b32 v249, 16, v106\n"
    "v_fmac_f32 v249, v248, v244\n"
    "v_and_b32 v248, 0xffff0000, v190\n"
    "v_and_b32 v250, 0xffff0000, v106\n"
    "v_fmac_f32 v250, v248, v245\n"
    "v_cvt_pk_bf16_f32 v106, v249, v250\n"
    "v_lshlrev_b32 v248, 16, v191\n"
    "v_lshlrev_b32 v249, 16, v107\n"
    "v_fmac_f32 v249, v248, v246\n"
    "v_and_b32 v248, 0xffff0000, v191\n"
    "v_and_b32 v250, 0xffff0000, v107\n"
    "v_fmac_f32 v250, v248, v247\n"
    "v_cvt_pk_bf16_f32 v107, v249, v250\n"
    "s_waitcnt lgkmcnt(10)\n"
    "v_accvgpr_read_b32 v244, a40\n"
    "v_accvgpr_read_b32 v245, a41\n"
    "v_accvgpr_read_b32 v246, a42\n"
    "v_accvgpr_read_b32 v247, a43\n"
    "v_lshlrev_b32 v248, 16, v192\n"
    "v_lshlrev_b32 v249, 16, v108\n"
    "v_fmac_f32 v249, v248, v244\n"
    "v_and_b32 v248, 0xffff0000, v192\n"
    "v_and_b32 v250, 0xffff0000, v108\n"
    "v_fmac_f32 v250, v248, v245\n"
    "v_cvt_pk_bf16_f32 v108, v249, v250\n"
    "v_lshlrev_b32 v248, 16, v193\n"
    "v_lshlrev_b32 v249, 16, v109\n"
    "v_fmac_f32 v249, v248, v246\n"
    "v_and_b32 v248, 0xffff0000, v193\n"
    "v_and_b32 v250, 0xffff0000, v109\n"
    "v_fmac_f32 v250, v248, v247\n"
    "v_cvt_pk_bf16_f32 v109, v249, v250\n"
    "s_waitcnt lgkmcnt(9)\n"
    "v_accvgpr_read_b32 v244, a56\n"
    "v_accvgpr_read_b32 v245, a57\n"
    "v_accvgpr_read_b32 v246, a58\n"
    "v_accvgpr_read_b32 v247, a59\n"
    "v_lshlrev_b32 v248, 16, v194\n"
    "v_lshlrev_b32 v249, 16, v110\n"
    "v_fmac_f32 v249, v248, v244\n"
    "v_and_b32 v248, 0xffff0000, v194\n"
    "v_and_b32 v250, 0xffff0000, v110\n"
    "v_fmac_f32 v250, v248, v245\n"
    "v_cvt_pk_bf16_f32 v110, v249, v250\n"
    "v_lshlrev_b32 v248, 16, v195\n"
    "v_lshlrev_b32 v249, 16, v111\n"
    "v_fmac_f32 v249, v248, v246\n"
    "v_and_b32 v248, 0xffff0000, v195\n"
    "v_and_b32 v250, 0xffff0000, v111\n"
    "v_fmac_f32 v250, v248, v247\n"
    "v_cvt_pk_bf16_f32 v111, v249, v250\n"
    "s_waitcnt lgkmcnt(8)\n"
    "v_accvgpr_read_b32 v244, a72\n"
    "v_accvgpr_read_b32 v245, a73\n"
    "v_accvgpr_read_b32 v246, a74\n"
    "v_accvgpr_read_b32 v247, a75\n"
    "v_lshlrev_b32 v248, 16, v196\n"
    "v_lshlrev_b32 v249, 16, v112\n"
    "v_fmac_f32 v249, v248, v244\n"
    "v_and_b32 v248, 0xffff0000, v196\n"
    "v_and_b32 v250, 0xffff0000, v112\n"
    "v_fmac_f32 v250, v248, v245\n"
    "v_cvt_pk_bf16_f32 v112, v249, v250\n"
    "v_lshlrev_b32 v248, 16, v197\n"
    "v_lshlrev_b32 v249, 16, v113\n"
    "v_fmac_f32 v249, v248, v246\n"
    "v_and_b32 v248, 0xffff0000, v197\n"
    "v_and_b32 v250, 0xffff0000, v113\n"
    "v_fmac_f32 v250, v248, v247\n"
    "v_cvt_pk_bf16_f32 v113, v249, v250\n"
    "s_waitcnt lgkmcnt(7)\n"
    "v_accvgpr_read_b32 v244, a88\n"
    "v_accvgpr_read_b32 v245, a89\n"
    "v_accvgpr_read_b32 v246, a90\n"
    "v_accvgpr_read_b32 v247, a91\n"
    "v_lshlrev_b32 v248, 16, v198\n"
    "v_lshlrev_b32 v249, 16, v114\n"
    "v_fmac_f32 v249, v248, v244\n"
    "v_and_b32 v248, 0xffff0000, v198\n"
    "v_and_b32 v250, 0xffff0000, v114\n"
    "v_fmac_f32 v250, v248, v245\n"
    "v_cvt_pk_bf16_f32 v114, v249, v250\n"
    "v_lshlrev_b32 v248, 16, v199\n"
    "v_lshlrev_b32 v249, 16, v115\n"
    "v_fmac_f32 v249, v248, v246\n"
    "v_and_b32 v248, 0xffff0000, v199\n"
    "v_and_b32 v250, 0xffff0000, v115\n"
    "v_fmac_f32 v250, v248, v247\n"
    "v_cvt_pk_bf16_f32 v115, v249, v250\n"
    "s_waitcnt lgkmcnt(6)\n"
    "v_accvgpr_read_b32 v244, a104\n"
    "v_accvgpr_read_b32 v245, a105\n"
    "v_accvgpr_read_b32 v246, a106\n"
    "v_accvgpr_read_b32 v247, a107\n"
    "v_lshlrev_b32 v248, 16, v200\n"
    "v_lshlrev_b32 v249, 16, v116\n"
    "v_fmac_f32 v249, v248, v244\n"
    "v_and_b32 v248, 0xffff0000, v200\n"
    "v_and_b32 v250, 0xffff0000, v116\n"
    "v_fmac_f32 v250, v248, v245\n"
    "v_cvt_pk_bf16_f32 v116, v249, v250\n"
    "v_lshlrev_b32 v248, 16, v201\n"
    "v_lshlrev_b32 v249, 16, v117\n"
    "v_fmac_f32 v249, v248, v246\n"
    "v_and_b32 v248, 0xffff0000, v201\n"
    "v_and_b32 v250, 0xffff0000, v117\n"
    "v_fmac_f32 v250, v248, v247\n"
    "v_cvt_pk_bf16_f32 v117, v249, v250\n"
    "s_waitcnt lgkmcnt(5)\n"
    "v_accvgpr_read_b32 v244, a120\n"
    "v_accvgpr_read_b32 v245, a121\n"
    "v_accvgpr_read_b32 v246, a122\n"
    "v_accvgpr_read_b32 v247, a123\n"
    "v_lshlrev_b32 v248, 16, v202\n"
    "v_lshlrev_b32 v249, 16, v118\n"
    "v_fmac_f32 v249, v248, v244\n"
    "v_and_b32 v248, 0xffff0000, v202\n"
    "v_and_b32 v250, 0xffff0000, v118\n"
    "v_fmac_f32 v250, v248, v245\n"
    "v_cvt_pk_bf16_f32 v118, v249, v250\n"
    "v_lshlrev_b32 v248, 16, v203\n"
    "v_lshlrev_b32 v249, 16, v119\n"
    "v_fmac_f32 v249, v248, v246\n"
    "v_and_b32 v248, 0xffff0000, v203\n"
    "v_and_b32 v250, 0xffff0000, v119\n"
    "v_fmac_f32 v250, v248, v247\n"
    "v_cvt_pk_bf16_f32 v119, v249, v250\n"
    "s_waitcnt lgkmcnt(4)\n"
    "v_accvgpr_read_b32 v244, a136\n"
    "v_accvgpr_read_b32 v245, a137\n"
    "v_accvgpr_read_b32 v246, a138\n"
    "v_accvgpr_read_b32 v247, a139\n"
    "v_lshlrev_b32 v248, 16, v204\n"
    "v_lshlrev_b32 v249, 16, v120\n"
    "v_fmac_f32 v249, v248, v244\n"
    "v_and_b32 v248, 0xffff0000, v204\n"
    "v_and_b32 v250, 0xffff0000, v120\n"
    "v_fmac_f32 v250, v248, v245\n"
    "v_cvt_pk_bf16_f32 v120, v249, v250\n"
    "v_lshlrev_b32 v248, 16, v205\n"
    "v_lshlrev_b32 v249, 16, v121\n"
    "v_fmac_f32 v249, v248, v246\n"
    "v_and_b32 v248, 0xffff0000, v205\n"
    "v_and_b32 v250, 0xffff0000, v121\n"
    "v_fmac_f32 v250, v248, v247\n"
    "v_cvt_pk_bf16_f32 v121, v249, v250\n"
    "s_waitcnt lgkmcnt(3)\n"
    "v_accvgpr_read_b32 v244, a152\n"
    "v_accvgpr_read_b32 v245, a153\n"
    "v_accvgpr_read_b32 v246, a154\n"
    "v_accvgpr_read_b32 v247, a155\n"
    "v_lshlrev_b32 v248, 16, v206\n"
    "v_lshlrev_b32 v249, 16, v122\n"
    "v_fmac_f32 v249, v248, v244\n"
    "v_and_b32 v248, 0xffff0000, v206\n"
    "v_and_b32 v250, 0xffff0000, v122\n"
    "v_fmac_f32 v250, v248, v245\n"
    "v_cvt_pk_bf16_f32 v122, v249, v250\n"
    "v_lshlrev_b32 v248, 16, v207\n"
    "v_lshlrev_b32 v249, 16, v123\n"
    "v_fmac_f32 v249, v248, v246\n"
    "v_and_b32 v248, 0xffff0000, v207\n"
    "v_and_b32 v250, 0xffff0000, v123\n"
    "v_fmac_f32 v250, v248, v247\n"
    "v_cvt_pk_bf16_f32 v123, v249, v250\n"
    "s_waitcnt lgkmcnt(2)\n"
    "v_accvgpr_read_b32 v244, a168\n"
    "v_accvgpr_read_b32 v245, a169\n"
    "v_accvgpr_read_b32 v246, a170\n"
    "v_accvgpr_read_b32 v247, a171\n"
    "v_lshlrev_b32 v248, 16, v208\n"
    "v_lshlrev_b32 v249, 16, v124\n"
    "v_fmac_f32 v249, v248, v244\n"
    "v_and_b32 v248, 0xffff0000, v208\n"
    "v_and_b32 v250, 0xffff0000, v124\n"
    "v_fmac_f32 v250, v248, v245\n"
    "v_cvt_pk_bf16_f32 v124, v249, v250\n"
    "v_lshlrev_b32 v248, 16, v209\n"
    "v_lshlrev_b32 v249, 16, v125\n"
    "v_fmac_f32 v249, v248, v246\n"
    "v_and_b32 v248, 0xffff0000, v209\n"
    "v_and_b32 v250, 0xffff0000, v125\n"
    "v_fmac_f32 v250, v248, v247\n"
    "v_cvt_pk_bf16_f32 v125, v249, v250\n"
    "s_waitcnt lgkmcnt(1)\n"
    "v_accvgpr_read_b32 v244, a184\n"
    "v_accvgpr_read_b32 v245, a185\n"
    "v_accvgpr_read_b32 v246, a186\n"
    "v_accvgpr_read_b32 v247, a187\n"
    "v_lshlrev_b32 v248, 16, v210\n"
    "v_lshlrev_b32 v249, 16, v126\n"
    "v_fmac_f32 v249, v248, v244\n"
    "v_and_b32 v248, 0xffff0000, v210\n"
    "v_and_b32 v250, 0xffff0000, v126\n"
    "v_fmac_f32 v250, v248, v245\n"
    "v_cvt_pk_bf16_f32 v126, v249, v250\n"
    "v_lshlrev_b32 v248, 16, v211\n"
    "v_lshlrev_b32 v249, 16, v127\n"
    "v_fmac_f32 v249, v248, v246\n"
    "v_and_b32 v248, 0xffff0000, v211\n"
    "v_and_b32 v250, 0xffff0000, v127\n"
    "v_fmac_f32 v250, v248, v247\n"
    "v_cvt_pk_bf16_f32 v127, v249, v250\n"
    "s_waitcnt lgkmcnt(0)\n"
    "v_accvgpr_read_b32 v244, a200\n"
    "v_accvgpr_read_b32 v245, a201\n"
    "v_accvgpr_read_b32 v246, a202\n"
    "v_accvgpr_read_b32 v247, a203\n"
    "v_lshlrev_b32 v248, 16, v212\n"
    "v_lshlrev_b32 v249, 16, v128\n"
    "v_fmac_f32 v249, v248, v244\n"
    "v_and_b32 v248, 0xffff0000, v212\n"
    "v_and_b32 v250, 0xffff0000, v128\n"
    "v_fmac_f32 v250, v248, v245\n"
    "v_cvt_pk_bf16_f32 v128, v249, v250\n"
    "v_lshlrev_b32 v248, 16, v213\n"
    "v_lshlrev_b32 v249, 16, v129\n"
    "v_fmac_f32 v249, v248, v246\n"
    "v_and_b32 v248, 0xffff0000, v213\n"
    "v_and_b32 v250, 0xffff0000, v129\n"
    "v_fmac_f32 v250, v248, v247\n"
    "v_cvt_pk_bf16_f32 v129, v249, v250\n"
    "global_store_dwordx2 v255, v[104:105], s[58:59] offset:0\n"
    "global_store_dwordx2 v255, v[106:107], s[58:59] offset:32\n"
    "global_store_dwordx2 v255, v[108:109], s[58:59] offset:64\n"
    "global_store_dwordx2 v255, v[110:111], s[58:59] offset:96\n"
    "global_store_dwordx2 v255, v[112:113], s[58:59] offset:128\n"
    "global_store_dwordx2 v255, v[114:115], s[58:59] offset:160\n"
    "global_store_dwordx2 v255, v[116:117], s[58:59] offset:192\n"
    "global_store_dwordx2 v255, v[118:119], s[58:59] offset:224\n"
    "global_store_dwordx2 v255, v[120:121], s[58:59] offset:256\n"
    "global_store_dwordx2 v255, v[122:123], s[58:59] offset:288\n"
    "global_store_dwordx2 v255, v[124:125], s[58:59] offset:320\n"
    "global_store_dwordx2 v255, v[126:127], s[58:59] offset:352\n"
    "global_store_dwordx2 v255, v[128:129], s[58:59] offset:384\n"
    "v_add_u32 v252, 80640, v254\n"
    "ds_read_b64 v[104:105], v252 offset:0\n"
    "ds_read_b64 v[106:107], v252 offset:32\n"
    "ds_read_b64 v[108:109], v252 offset:64\n"
    "ds_read_b64 v[110:111], v252 offset:96\n"
    "ds_read_b64 v[112:113], v252 offset:128\n"
    "ds_read_b64 v[114:115], v252 offset:160\n"
    "ds_read_b64 v[116:117], v252 offset:192\n"
    "ds_read_b64 v[118:119], v252 offset:224\n"
    "ds_read_b64 v[120:121], v252 offset:256\n"
    "ds_read_b64 v[122:123], v252 offset:288\n"
    "ds_read_b64 v[124:125], v252 offset:320\n"
    "ds_read_b64 v[126:127], v252 offset:352\n"
    "ds_read_b64 v[128:129], v252 offset:384\n"
    "s_add_u32 s58, s54, 79872\n"
    "s_addc_u32 s59, s55, 0\n"
    "s_waitcnt lgkmcnt(12)\n"
    "v_accvgpr_read_b32 v244, a12\n"
    "v_accvgpr_read_b32 v245, a13\n"
    "v_accvgpr_read_b32 v246, a14\n"
    "v_accvgpr_read_b32 v247, a15\n"
    "v_lshlrev_b32 v248, 16, v214\n"
    "v_lshlrev_b32 v249, 16, v104\n"
    "v_fmac_f32 v249, v248, v244\n"
    "v_and_b32 v248, 0xffff0000, v214\n"
    "v_and_b32 v250, 0xffff0000, v104\n"
    "v_fmac_f32 v250, v248, v245\n"
    "v_cvt_pk_bf16_f32 v104, v249, v250\n"
    "v_lshlrev_b32 v248, 16, v215\n"
    "v_lshlrev_b32 v249, 16, v105\n"
    "v_fmac_f32 v249, v248, v246\n"
    "v_and_b32 v248, 0xffff0000, v215\n"
    "v_and_b32 v250, 0xffff0000, v105\n"
    "v_fmac_f32 v250, v248, v247\n"
    "v_cvt_pk_bf16_f32 v105, v249, v250\n"
    "s_waitcnt lgkmcnt(11)\n"
    "v_accvgpr_read_b32 v244, a28\n"
    "v_accvgpr_read_b32 v245, a29\n"
    "v_accvgpr_read_b32 v246, a30\n"
    "v_accvgpr_read_b32 v247, a31\n"
    "v_lshlrev_b32 v248, 16, v216\n"
    "v_lshlrev_b32 v249, 16, v106\n"
    "v_fmac_f32 v249, v248, v244\n"
    "v_and_b32 v248, 0xffff0000, v216\n"
    "v_and_b32 v250, 0xffff0000, v106\n"
    "v_fmac_f32 v250, v248, v245\n"
    "v_cvt_pk_bf16_f32 v106, v249, v250\n"
    "v_lshlrev_b32 v248, 16, v217\n"
    "v_lshlrev_b32 v249, 16, v107\n"
    "v_fmac_f32 v249, v248, v246\n"
    "v_and_b32 v248, 0xffff0000, v217\n"
    "v_and_b32 v250, 0xffff0000, v107\n"
    "v_fmac_f32 v250, v248, v247\n"
    "v_cvt_pk_bf16_f32 v107, v249, v250\n"
    "s_waitcnt lgkmcnt(10)\n"
    "v_accvgpr_read_b32 v244, a44\n"
    "v_accvgpr_read_b32 v245, a45\n"
    "v_accvgpr_read_b32 v246, a46\n"
    "v_accvgpr_read_b32 v247, a47\n"
    "v_lshlrev_b32 v248, 16, v218\n"
    "v_lshlrev_b32 v249, 16, v108\n"
    "v_fmac_f32 v249, v248, v244\n"
    "v_and_b32 v248, 0xffff0000, v218\n"
    "v_and_b32 v250, 0xffff0000, v108\n"
    "v_fmac_f32 v250, v248, v245\n"
    "v_cvt_pk_bf16_f32 v108, v249, v250\n"
    "v_lshlrev_b32 v248, 16, v219\n"
    "v_lshlrev_b32 v249, 16, v109\n"
    "v_fmac_f32 v249, v248, v246\n"
    "v_and_b32 v248, 0xffff0000, v219\n"
    "v_and_b32 v250, 0xffff0000, v109\n"
    "v_fmac_f32 v250, v248, v247\n"
    "v_cvt_pk_bf16_f32 v109, v249, v250\n"
    "s_waitcnt lgkmcnt(9)\n"
    "v_accvgpr_read_b32 v244, a60\n"
    "v_accvgpr_read_b32 v245, a61\n"
    "v_accvgpr_read_b32 v246, a62\n"
    "v_accvgpr_read_b32 v247, a63\n"
    "v_lshlrev_b32 v248, 16, v220\n"
    "v_lshlrev_b32 v249, 16, v110\n"
    "v_fmac_f32 v249, v248, v244\n"
    "v_and_b32 v248, 0xffff0000, v220\n"
    "v_and_b32 v250, 0xffff0000, v110\n"
    "v_fmac_f32 v250, v248, v245\n"
    "v_cvt_pk_bf16_f32 v110, v249, v250\n"
    "v_lshlrev_b32 v248, 16, v221\n"
    "v_lshlrev_b32 v249, 16, v111\n"
    "v_fmac_f32 v249, v248, v246\n"
    "v_and_b32 v248, 0xffff0000, v221\n"
    "v_and_b32 v250, 0xffff0000, v111\n"
    "v_fmac_f32 v250, v248, v247\n"
    "v_cvt_pk_bf16_f32 v111, v249, v250\n"
    "s_waitcnt lgkmcnt(8)\n"
    "v_accvgpr_read_b32 v244, a76\n"
    "v_accvgpr_read_b32 v245, a77\n"
    "v_accvgpr_read_b32 v246, a78\n"
    "v_accvgpr_read_b32 v247, a79\n"
    "v_lshlrev_b32 v248, 16, v222\n"
    "v_lshlrev_b32 v249, 16, v112\n"
    "v_fmac_f32 v249, v248, v244\n"
    "v_and_b32 v248, 0xffff0000, v222\n"
    "v_and_b32 v250, 0xffff0000, v112\n"
    "v_fmac_f32 v250, v248, v245\n"
    "v_cvt_pk_bf16_f32 v112, v249, v250\n"
    "v_lshlrev_b32 v248, 16, v223\n"
    "v_lshlrev_b32 v249, 16, v113\n"
    "v_fmac_f32 v249, v248, v246\n"
    "v_and_b32 v248, 0xffff0000, v223\n"
    "v_and_b32 v250, 0xffff0000, v113\n"
    "v_fmac_f32 v250, v248, v247\n"
    "v_cvt_pk_bf16_f32 v113, v249, v250\n"
    "s_waitcnt lgkmcnt(7)\n"
    "v_accvgpr_read_b32 v244, a92\n"
    "v_accvgpr_read_b32 v245, a93\n"
    "v_accvgpr_read_b32 v246, a94\n"
    "v_accvgpr_read_b32 v247, a95\n"
    "v_lshlrev_b32 v248, 16, v224\n"
    "v_lshlrev_b32 v249, 16, v114\n"
    "v_fmac_f32 v249, v248, v244\n"
    "v_and_b32 v248, 0xffff0000, v224\n"
    "v_and_b32 v250, 0xffff0000, v114\n"
    "v_fmac_f32 v250, v248, v245\n"
    "v_cvt_pk_bf16_f32 v114, v249, v250\n"
    "v_lshlrev_b32 v248, 16, v225\n"
    "v_lshlrev_b32 v249, 16, v115\n"
    "v_fmac_f32 v249, v248, v246\n"
    "v_and_b32 v248, 0xffff0000, v225\n"
    "v_and_b32 v250, 0xffff0000, v115\n"
    "v_fmac_f32 v250, v248, v247\n"
    "v_cvt_pk_bf16_f32 v115, v249, v250\n"
    "s_waitcnt lgkmcnt(6)\n"
    "v_accvgpr_read_b32 v244, a108\n"
    "v_accvgpr_read_b32 v245, a109\n"
    "v_accvgpr_read_b32 v246, a110\n"
    "v_accvgpr_read_b32 v247, a111\n"
    "v_lshlrev_b32 v248, 16, v226\n"
    "v_lshlrev_b32 v249, 16, v116\n"
    "v_fmac_f32 v249, v248, v244\n"
    "v_and_b32 v248, 0xffff0000, v226\n"
    "v_and_b32 v250, 0xffff0000, v116\n"
    "v_fmac_f32 v250, v248, v245\n"
    "v_cvt_pk_bf16_f32 v116, v249, v250\n"
    "v_lshlrev_b32 v248, 16, v227\n"
    "v_lshlrev_b32 v249, 16, v117\n"
    "v_fmac_f32 v249, v248, v246\n"
    "v_and_b32 v248, 0xffff0000, v227\n"
    "v_and_b32 v250, 0xffff0000, v117\n"
    "v_fmac_f32 v250, v248, v247\n"
    "v_cvt_pk_bf16_f32 v117, v249, v250\n"
    "s_waitcnt lgkmcnt(5)\n"
    "v_accvgpr_read_b32 v244, a124\n"
    "v_accvgpr_read_b32 v245, a125\n"
    "v_accvgpr_read_b32 v246, a126\n"
    "v_accvgpr_read_b32 v247, a127\n"
    "v_lshlrev_b32 v248, 16, v228\n"
    "v_lshlrev_b32 v249, 16, v118\n"
    "v_fmac_f32 v249, v248, v244\n"
    "v_and_b32 v248, 0xffff0000, v228\n"
    "v_and_b32 v250, 0xffff0000, v118\n"
    "v_fmac_f32 v250, v248, v245\n"
    "v_cvt_pk_bf16_f32 v118, v249, v250\n"
    "v_lshlrev_b32 v248, 16, v229\n"
    "v_lshlrev_b32 v249, 16, v119\n"
    "v_fmac_f32 v249, v248, v246\n"
    "v_and_b32 v248, 0xffff0000, v229\n"
    "v_and_b32 v250, 0xffff0000, v119\n"
    "v_fmac_f32 v250, v248, v247\n"
    "v_cvt_pk_bf16_f32 v119, v249, v250\n"
    "s_waitcnt lgkmcnt(4)\n"
    "v_accvgpr_read_b32 v244, a140\n"
    "v_accvgpr_read_b32 v245, a141\n"
    "v_accvgpr_read_b32 v246, a142\n"
    "v_accvgpr_read_b32 v247, a143\n"
    "v_lshlrev_b32 v248, 16, v230\n"
    "v_lshlrev_b32 v249, 16, v120\n"
    "v_fmac_f32 v249, v248, v244\n"
    "v_and_b32 v248, 0xffff0000, v230\n"
    "v_and_b32 v250, 0xffff0000, v120\n"
    "v_fmac_f32 v250, v248, v245\n"
    "v_cvt_pk_bf16_f32 v120, v249, v250\n"
    "v_lshlrev_b32 v248, 16, v231\n"
    "v_lshlrev_b32 v249, 16, v121\n"
    "v_fmac_f32 v249, v248, v246\n"
    "v_and_b32 v248, 0xffff0000, v231\n"
    "v_and_b32 v250, 0xffff0000, v121\n"
    "v_fmac_f32 v250, v248, v247\n"
    "v_cvt_pk_bf16_f32 v121, v249, v250\n"
    "s_waitcnt lgkmcnt(3)\n"
    "v_accvgpr_read_b32 v244, a156\n"
    "v_accvgpr_read_b32 v245, a157\n"
    "v_accvgpr_read_b32 v246, a158\n"
    "v_accvgpr_read_b32 v247, a159\n"
    "v_lshlrev_b32 v248, 16, v232\n"
    "v_lshlrev_b32 v249, 16, v122\n"
    "v_fmac_f32 v249, v248, v244\n"
    "v_and_b32 v248, 0xffff0000, v232\n"
    "v_and_b32 v250, 0xffff0000, v122\n"
    "v_fmac_f32 v250, v248, v245\n"
    "v_cvt_pk_bf16_f32 v122, v249, v250\n"
    "v_lshlrev_b32 v248, 16, v233\n"
    "v_lshlrev_b32 v249, 16, v123\n"
    "v_fmac_f32 v249, v248, v246\n"
    "v_and_b32 v248, 0xffff0000, v233\n"
    "v_and_b32 v250, 0xffff0000, v123\n"
    "v_fmac_f32 v250, v248, v247\n"
    "v_cvt_pk_bf16_f32 v123, v249, v250\n"
    "s_waitcnt lgkmcnt(2)\n"
    "v_accvgpr_read_b32 v244, a172\n"
    "v_accvgpr_read_b32 v245, a173\n"
    "v_accvgpr_read_b32 v246, a174\n"
    "v_accvgpr_read_b32 v247, a175\n"
    "v_lshlrev_b32 v248, 16, v234\n"
    "v_lshlrev_b32 v249, 16, v124\n"
    "v_fmac_f32 v249, v248, v244\n"
    "v_and_b32 v248, 0xffff0000, v234\n"
    "v_and_b32 v250, 0xffff0000, v124\n"
    "v_fmac_f32 v250, v248, v245\n"
    "v_cvt_pk_bf16_f32 v124, v249, v250\n"
    "v_lshlrev_b32 v248, 16, v235\n"
    "v_lshlrev_b32 v249, 16, v125\n"
    "v_fmac_f32 v249, v248, v246\n"
    "v_and_b32 v248, 0xffff0000, v235\n"
    "v_and_b32 v250, 0xffff0000, v125\n"
    "v_fmac_f32 v250, v248, v247\n"
    "v_cvt_pk_bf16_f32 v125, v249, v250\n"
    "s_waitcnt lgkmcnt(1)\n"
    "v_accvgpr_read_b32 v244, a188\n"
    "v_accvgpr_read_b32 v245, a189\n"
    "v_accvgpr_read_b32 v246, a190\n"
    "v_accvgpr_read_b32 v247, a191\n"
    "v_lshlrev_b32 v248, 16, v236\n"
    "v_lshlrev_b32 v249, 16, v126\n"
    "v_fmac_f32 v249, v248, v244\n"
    "v_and_b32 v248, 0xffff0000, v236\n"
    "v_and_b32 v250, 0xffff0000, v126\n"
    "v_fmac_f32 v250, v248, v245\n"
    "v_cvt_pk_bf16_f32 v126, v249, v250\n"
    "v_lshlrev_b32 v248, 16, v237\n"
    "v_lshlrev_b32 v249, 16, v127\n"
    "v_fmac_f32 v249, v248, v246\n"
    "v_and_b32 v248, 0xffff0000, v237\n"
    "v_and_b32 v250, 0xffff0000, v127\n"
    "v_fmac_f32 v250, v248, v247\n"
    "v_cvt_pk_bf16_f32 v127, v249, v250\n"
    "s_waitcnt lgkmcnt(0)\n"
    "v_accvgpr_read_b32 v244, a204\n"
    "v_accvgpr_read_b32 v245, a205\n"
    "v_accvgpr_read_b32 v246, a206\n"
    "v_accvgpr_read_b32 v247, a207\n"
    "v_lshlrev_b32 v248, 16, v238\n"
    "v_lshlrev_b32 v249, 16, v128\n"
    "v_fmac_f32 v249, v248, v244\n"
    "v_and_b32 v248, 0xffff0000, v238\n"
    "v_and_b32 v250, 0xffff0000, v128\n"
    "v_fmac_f32 v250, v248, v245\n"
    "v_cvt_pk_bf16_f32 v128, v249, v250\n"
    "v_lshlrev_b32 v248, 16, v239\n"
    "v_lshlrev_b32 v249, 16, v129\n"
    "v_fmac_f32 v249, v248, v246\n"
    "v_and_b32 v248, 0xffff0000, v239\n"
    "v_and_b32 v250, 0xffff0000, v129\n"
    "v_fmac_f32 v250, v248, v247\n"
    "v_cvt_pk_bf16_f32 v129, v249, v250\n"
    "global_store_dwordx2 v255, v[104:105], s[58:59] offset:0\n"
    "global_store_dwordx2 v255, v[106:107], s[58:59] offset:32\n"
    "global_store_dwordx2 v255, v[108:109], s[58:59] offset:64\n"
    "global_store_dwordx2 v255, v[110:111], s[58:59] offset:96\n"
    "global_store_dwordx2 v255, v[112:113], s[58:59] offset:128\n"
    "global_store_dwordx2 v255, v[114:115], s[58:59] offset:160\n"
    "global_store_dwordx2 v255, v[116:117], s[58:59] offset:192\n"
    "global_store_dwordx2 v255, v[118:119], s[58:59] offset:224\n"
    "global_store_dwordx2 v255, v[120:121], s[58:59] offset:256\n"
    "global_store_dwordx2 v255, v[122:123], s[58:59] offset:288\n"
    "global_store_dwordx2 v255, v[124:125], s[58:59] offset:320\n"
    "global_store_dwordx2 v255, v[126:127], s[58:59] offset:352\n"
    "global_store_dwordx2 v255, v[128:129], s[58:59] offset:384\n"
    :: "s"(pw), "s"(px), "s"(bs), "s"(po), "s"(wave)
    : "v0","v1","v2","v3","v4","v5","v6","v7","v8","v9","v10","v11","v12","v13","v14","v15","v16","v17","v18","v19","v20","v21","v22","v23","v24","v25","v26","v27","v28","v29","v30","v31","v32","v33","v34","v35","v36","v37","v38","v39","v40","v41","v42","v43","v44","v45","v46","v47","v48","v49","v50","v51","v52","v53","v54","v55","v56","v57","v58","v59","v60","v61","v62","v63","v64","v65","v66","v67","v68","v69","v70","v71","v72","v73","v74","v75","v76","v77","v78","v79","v80","v81","v82","v83","v84","v85","v86","v87","v88","v89","v90","v91","v92","v93","v94","v95","v96","v97","v98","v99","v100","v101","v102","v103","v104","v105","v106","v107","v108","v109","v110","v111","v112","v113","v114","v115","v116","v117","v118","v119","v120","v121","v122","v123","v124","v125","v126","v127","v128","v129","v130","v131","v132","v133","v134","v135","v136","v137","v138","v139","v140","v141","v142","v143","v144","v145","v146","v147","v148","v149","v150","v151","v152","v153","v154","v155","v156","v157","v158","v159","v160","v161","v162","v163","v164","v165","v166","v167","v168","v169","v170","v171","v172","v173","v174","v175","v176","v177","v178","v179","v180","v181","v182","v183","v184","v185","v186","v187","v188","v189","v190","v191","v192","v193","v194","v195","v196","v197","v198","v199","v200","v201","v202","v203","v204","v205","v206","v207","v208","v209","v210","v211","v212","v213","v214","v215","v216","v217","v218","v219","v220","v221","v222","v223","v224","v225","v226","v227","v228","v229","v230","v231","v232","v233","v234","v235","v236","v237","v238","v239","v240","v241","v242","v243","v244","v245","v246","v247","v248","v249","v250","v251","v252","v253","v254","v255","a0","a1","a2","a3","a4","a5","a6","a7","a8","a9","a10","a11","a12","a13","a14","a15","a16","a17","a18","a19","a20","a21","a22","a23","a24","a25","a26","a27","a28","a29","a30","a31","a32","a33","a34","a35","a36","a37","a38","a39","a40","a41","a42","a43","a44","a45","a46","a47","a48","a49","a50","a51","a52","a53","a54","a55","a56","a57","a58","a59","a60","a61","a62","a63","a64","a65","a66","a67","a68","a69","a70","a71","a72","a73","a74","a75","a76","a77","a78","a79","a80","a81","a82","a83","a84","a85","a86","a87","a88","a89","a90","a91","a92","a93","a94","a95","a96","a97","a98","a99","a100","a101","a102","a103","a104","a105","a106","a107","a108","a109","a110","a111","a112","a113","a114","a115","a116","a117","a118","a119","a120","a121","a122","a123","a124","a125","a126","a127","a128","a129","a130","a131","a132","a133","a134","a135","a136","a137","a138","a139","a140","a141","a142","a143","a144","a145","a146","a147","a148","a149","a150","a151","a152","a153","a154","a155","a156","a157","a158","a159","a160","a161","a162","a163","a164","a165","a166","a167","a168","a169","a170","a171","a172","a173","a174","a175","a176","a177","a178","a179","a180","a181","a182","a183","a184","a185","a186","a187","a188","a189","a190","a191","a192","a193","a194","a195","a196","a197","a198","a199","a200","a201","a202","a203","a204","a205","a206","a207","a208","a209","a210","a211","a212","a213","a214","a215","a216","a217","a218","a219","a220","a221","a222","a223","a224","a225","a226","a227","a228","a229","a230","a231","a232","a233","a234","a235","a236","a237","a238","a239","a240","a241","a242","a243","a244","a245","a246","a247","a248","a249","a250","a251","a252","a253","a254","a255","s40","s41","s42","s43","s44","s45","s46","s47","s48","s49","s50","s51","s52","s53","s54","s55","s56","s57","s58","s59","s60","s61","s62","s63","s64","s65","s66","s67","s68","s69","s70","s71","s72","s73","s74","s75","s76","s77","s78","s79","s80","s81","s82","s83","s84","s85","s86","s87","s88","s89","s90","s91","s92","s93","s94","s95","s96","s97","s98","s99","vcc","scc","memory");
}

extern "C" void kernel_launch(void* const* d_in, const int* in_sizes, int n_in,
                              void* d_out, int out_size, void* d_ws, size_t ws_size,
                              hipStream_t stream) {
    const unsigned short* x0 = (const unsigned short*)d_in[0];
    const unsigned short* ws = (const unsigned short*)d_in[1];
    const char* bs = (const char*)d_in[2];
    const int B = in_sizes[0] / DIM;
    char* P = (char*)d_ws;
    const int pack_threads = 4 * FRAGS_PER_WAVE * 64;
    dcn_pack_w<<<pack_threads / 256, 256, 0, stream>>>(ws, (u32x4*)P);
    dcn_fused<<<B / ROWS, 256, 0, stream>>>(P, (const char*)x0, bs, (char*)d_out);
}
```
